```python
import torch

def _softmax(x, axis=-1):
    return torch.softmax(x, dim=axis)

def _gelu(x):
    return torch.nn.functional.gelu(x, approximate='tanh')

def _silu(x):
    return torch.nn.functional.silu(x)

def _sigmoid(x):
    return torch.sigmoid(x)

def _layernorm(x, axis=-1, eps=1e-05):
    m = x.mean(dim=axis, keepdim=True)
    v = x.var(dim=axis, keepdim=True, unbiased=False)
    return (x - m) / torch.sqrt(v + eps)

def _scatter_add(tgt, idx, src, axis=0):
    return tgt.clone().index_add_(axis, idx, src)

def _segment_sum(src, seg, n):
    out = torch.zeros((n,) + src.shape[1:], dtype=src.dtype)
    return out.index_add_(0, seg, src)

def _pad(x, pw):
    flat = tuple((v for pair in reversed(pw) for v in pair))
    return torch.nn.functional.pad(x, flat)
kernel_name = 'adaLN_cond__mha_block__moe_arch_mod__prompt'

def setup_inputs():

    _w = lambda a: (lambda t: t.to(torch.bfloat16) if t.is_floating_point() else t)(torch.as_tensor(a))

    def _A_setup():
        B, S, D, H = (1, 2048, 512, 32)
        return {'x': _w((lambda *s: torch.randn(*s, dtype=torch.bfloat16) * 0.05)(B, S, D)), 'wqkv': _w((lambda *s: torch.randn(*s, dtype=torch.bfloat16) * 0.05)(D, 3 * D)), 'wo': _w((lambda *s: torch.randn(*s, dtype=torch.bfloat16) * 0.05)(D, D))}
    _A_in = _A_setup()

    def _B_setup():
        B, S, D, F, C = (1, 2048, 512, 1792, 2)
        return {'x': _w((lambda *s: torch.randn(*s, dtype=torch.bfloat16) * 0.05)(B, S, D)), 'w_r': _w((lambda *s: torch.randn(*s, dtype=torch.bfloat16) * 0.05)(D)), 'w_gate': _w((lambda *s: torch.randn(*s, dtype=torch.bfloat16) * 0.05)(D, F)), 'w_up': _w((lambda *s: torch.randn(*s, dtype=torch.bfloat16) * 0.05)(D, F)), 'w_down': _w((lambda *s: torch.randn(*s, dtype=torch.bfloat16) * 0.05)(F, D))}
    _B_in = _B_setup()
    return {'A_x': _A_in['x'], 'A_wqkv': _A_in['wqkv'], 'A_wo': _A_in['wo'], 'B_x': _B_in['x'], 'B_w_r': _B_in['w_r'], 'B_w_gate': _B_in['w_gate'], 'B_w_up': _B_in['w_up'], 'B_w_down': _B_in['w_down'], 'c': _w((lambda *s: torch.randn(*s, dtype=torch.bfloat16) * 0.1)(1, 512) * 8.0), 'w_mod': _w((lambda *s: torch.randn(*s, dtype=torch.bfloat16) * 0.1)(512, 6 * 512) * 512 ** (-0.5) * 8.0)}

def reference(A_x, A_wqkv, A_wo, B_x, B_w_r, B_w_gate, B_w_up, B_w_down, c, w_mod):

    def _A_ref(x, wqkv, wo):
        B, S, D = x.shape
        H = 32
        Dh = D // H
        qkv = (lambda x, *d: x.permute(*d))((x @ wqkv).reshape(B, S, 3, H, Dh), 2, 0, 3, 1, 4)
        q, k, v = (qkv[0], qkv[1], qkv[2])
        scores = torch.einsum('bhqd,bhkd->bhqk', q, k) * Dh ** (-0.5)
        p = _softmax(scores, axis=-1)
        o = torch.einsum('bhqk,bhkd->bhqd', p, v)
        return (lambda x, *d: x.permute(*d))(o, 0, 2, 1, 3).reshape(B, S, D) @ wo

    def _B_ref(x, w_r, w_gate, w_up, w_down):
        B, S, D = x.shape
        C = 2
        r = x @ w_r
        idx = torch.argsort(-r, axis=-1)[:, :C]
        idx_e = (lambda x, n, axis: torch.repeat_interleave(x, n, dim=axis))(idx.reshape(B, C, 1), D, axis=2)
        x_sel = (lambda x, i, axis: torch.take_along_dim(x, i, dim=axis))(x, idx_e, axis=1)
        g = _silu(x_sel @ w_gate)
        u = x_sel @ w_up
        y = g * u @ w_down
        r_sel = _sigmoid((lambda x, i, axis: torch.take_along_dim(x, i, dim=axis))(r, idx, axis=1))
        y = y * r_sel.reshape(B, C, 1)
        b_off = (torch.arange(B) * S).reshape(B, 1)
        flat_idx = (idx + b_off).reshape(B * C)
        zero = (x * 0.0).reshape(B * S, D)
        upd = _scatter_add(zero, flat_idx, y.reshape(B * C, D), axis=0)
        return x + upd.reshape(B, S, D)
    x = A_x
    mod = (_silu(c) @ w_mod).reshape(c.shape[0], 6, 1, -1)
    sa, ca, ga, sb, cb, gb = (mod[:, 0].clone(), mod[:, 1].clone(), mod[:, 2].clone(), mod[:, 3].clone(), mod[:, 4].clone(), mod[:, 5].clone())
    h = _layernorm(x, axis=-1) * (1 + ca) + sa
    x = x + ga * _A_ref(h, A_wqkv, A_wo)
    h = _layernorm(x, axis=-1) * (1 + cb) + sb
    x = x + gb * _B_ref(h, B_w_r, B_w_gate, B_w_up, B_w_down)
    return x
```

```cpp
#include <hip/hip_runtime.h>
#include <hip/hip_bf16.h>
#include <stdint.h>
typedef unsigned short u16;
__device__ __forceinline__ float b2f(u16 v){ return __uint_as_float(((unsigned)v)<<16); }
__device__ __forceinline__ u16 f2b(float f){ __hip_bfloat16 b=__float2bfloat16(f); return *(u16*)&b; }
__device__ __forceinline__ float rb(float f){ return b2f(f2b(f)); }
#define S 2048
#define D 512
#define F 1792
__global__ void k_mod(const u16* c,const u16* wm,float* mod){
  int j=blockIdx.x*blockDim.x+threadIdx.x; float a=0;
  for(int i=0;i<D;i++){ float x=b2f(c[i]); float s=rb(x/(1.f+__expf(-x))); a+=s*b2f(wm[i*3072+j]); }
  mod[j]=rb(a);
}
__global__ void k_ln(const u16* x,const float* sh,const float* sc,u16* h){
  int t=blockIdx.x; float v[8]; float s=0; for(int i=0;i<8;i++){ v[i]=b2f(x[t*D+threadIdx.x*8+i]); s+=v[i]; }
  for(int o=32;o;o>>=1) s+=__shfl_xor(s,o); float m=s/D; float q=0; for(int i=0;i<8;i++){ q+=(v[i]-m)*(v[i]-m);}
  for(int o=32;o;o>>=1) q+=__shfl_xor(q,o); float r=rsqrtf(q/D+1e-5f);
  for(int i=0;i<8;i++){ int j=threadIdx.x*8+i; h[t*D+j]=f2b((v[i]-m)*r*(1.f+sc[j])+sh[j]); }
}
__global__ void k_gemm(const u16* A,const u16* B,u16* C,int N,int K){
  int n=blockIdx.x*blockDim.x+threadIdx.x; int m=blockIdx.y; float a=0;
  for(int k=0;k<K;k++) a+=b2f(A[m*K+k])*b2f(B[k*N+n]);
  C[m*N+n]=f2b(a);
}
__global__ void k_attn(const u16* qkv,u16* o){
  int q=blockIdx.x*blockDim.x+threadIdx.x; int h=blockIdx.y; float qq[16]; for(int d=0;d<16;d++) qq[d]=b2f(qkv[q*1536+h*16+d])*0.25f;
  float m=-1e30f,l=0,acc[16]; for(int d=0;d<16;d++) acc[d]=0;
  for(int k=0;k<S;k++){ float s=0; for(int d=0;d<16;d++) s+=qq[d]*b2f(qkv[k*1536+512+h*16+d]);
    float mn=fmaxf(m,s); float a=__expf(m-mn); float p=__expf(s-mn); l=l*a+p; for(int d=0;d<16;d++) acc[d]=acc[d]*a+p*b2f(qkv[k*1536+1024+h*16+d]); m=mn; }
  for(int d=0;d<16;d++) o[q*D+h*16+d]=f2b(acc[d]/l);
}
__global__ void k_res(const u16* x,const u16* a,const float* ga,u16* x1){ int i=blockIdx.x*blockDim.x+threadIdx.x; x1[i]=f2b(b2f(x[i])+ga[i%D]*b2f(a[i])); }
__global__ void k_r(const u16* h,const u16* wr,float* r){ int t=blockIdx.x; float s=0; for(int i=0;i<8;i++){int j=threadIdx.x*8+i; s+=b2f(h[t*D+j])*b2f(wr[j]);} for(int o=32;o;o>>=1) s+=__shfl_xor(s,o); if(threadIdx.x==0) r[t]=s; }
__global__ void k_top(const float* r,int* idx){ if(threadIdx.x==0){ int i0=0; for(int t=1;t<S;t++) if(r[t]>r[i0]) i0=t; int i1=(i0==0)?1:0; for(int t=0;t<S;t++) if(t!=i0&&r[t]>r[i1]) i1=t; idx[0]=i0; idx[1]=i1; } }
__global__ void k_gu(const u16* h,const int* idx,const u16* wg,const u16* wu,float* gu){ int f=blockIdx.x*blockDim.x+threadIdx.x; int c=blockIdx.y; int t=idx[c]; float g=0,u=0; for(int i=0;i<D;i++){ float x=b2f(h[t*D+i]); g+=x*b2f(wg[i*F+f]); u+=x*b2f(wu[i*F+f]); } g=rb(g); g=rb(g/(1.f+__expf(-g))); gu[c*F+f]=rb(g*rb(u)); }
__global__ void k_out(const u16* x1,const u16* h,const float* gb,u16* out){ int i=blockIdx.x*blockDim.x+threadIdx.x; out[i]=f2b(b2f(x1[i])+gb[i%D]*b2f(h[i])); }
__global__ void k_down(const u16* x1,const u16* h,const float* gb,const float* gu,const u16* wd,const float* r,const int* idx,u16* out){ int j=blockIdx.x*blockDim.x+threadIdx.x; int c=blockIdx.y; int t=idx[c]; float y=0; for(int f=0;f<F;f++) y+=gu[c*F+f]*b2f(wd[f*D+j]); float sg=1.f/(1.f+__expf(-rb(r[t]))); y=rb(y)*sg; out[t*D+j]=f2b(b2f(x1[t*D+j])+gb[j]*(b2f(h[t*D+j])+y)); }
extern "C" void kernel_launch(void* const* d_in, const int* in_sizes, int n_in, void* d_out, int out_size, void* d_ws, size_t ws_size, hipStream_t stream) {
  const u16* x=(const u16*)d_in[0]; const u16* wqkv=(const u16*)d_in[1]; const u16* wo=(const u16*)d_in[2];
  const u16* wr=(const u16*)d_in[4]; const u16* wg=(const u16*)d_in[5]; const u16* wu=(const u16*)d_in[6]; const u16* wd=(const u16*)d_in[7];
  const u16* c=(const u16*)d_in[8]; const u16* wm=(const u16*)d_in[9]; u16* out=(u16*)d_out;
  char* ws=(char*)d_ws; float* mod=(float*)ws; u16* h=(u16*)(ws+(1<<20)); u16* qkv=(u16*)(ws+(4<<20)); u16* o=(u16*)(ws+(12<<20)); u16* a=(u16*)(ws+(16<<20)); u16* x1=(u16*)(ws+(20<<20)); u16* h2=(u16*)(ws+(24<<20)); float* r=(float*)(ws+(28<<20)); int* idx=(int*)(ws+(29<<20)); float* gu=(float*)(ws+(30<<20));
  k_mod<<<12,256,0,stream>>>(c,wm,mod);
  k_ln<<<S,64,0,stream>>>(x,mod,mod+512,h);
  k_gemm<<<dim3(6,S),256,0,stream>>>(h,wqkv,qkv,1536,512);
  k_attn<<<dim3(8,32),256,0,stream>>>(qkv,o);
  k_gemm<<<dim3(2,S),256,0,stream>>>(o,wo,a,512,512);
  k_res<<<S*D/256,256,0,stream>>>(x,a,mod+1024,x1);
  k_ln<<<S,64,0,stream>>>(x1,mod+1536,mod+2048,h2);
  k_r<<<S,64,0,stream>>>(h2,wr,r);
  k_top<<<1,64,0,stream>>>(r,idx);
  k_gu<<<dim3(7,2),256,0,stream>>>(h2,idx,wg,wu,gu);
  k_out<<<S*D/256,256,0,stream>>>(x1,h2,mod+2560,out);
  k_down<<<dim3(2,2),256,0,stream>>>(x1,h2,mod+2560,gu,wd,r,idx,out);
}
```

```cpp
#include <hip/hip_runtime.h>
#include <hip/hip_bf16.h>
#include <stdint.h>
typedef unsigned short u16;
typedef unsigned long long u64;
typedef float f32x2 __attribute__((ext_vector_type(2)));
typedef float f32x4 __attribute__((ext_vector_type(4)));
typedef float f32x16 __attribute__((ext_vector_type(16)));
typedef __bf16 bf16x2 __attribute__((ext_vector_type(2)));
typedef __bf16 bf16x8 __attribute__((ext_vector_type(8)));
typedef unsigned v4u __attribute__((ext_vector_type(4)));
typedef unsigned v2u __attribute__((ext_vector_type(2)));
typedef unsigned v16u __attribute__((ext_vector_type(16)));
typedef unsigned v32u __attribute__((ext_vector_type(32)));
typedef short s16x4 __attribute__((ext_vector_type(4)));
#define LAS __attribute__((address_space(3)))
#define GAS __attribute__((address_space(1)))
#define DEV __device__ __forceinline__

#define ATTN_A \
  "ds_read_b128 v[48:51], v73 offset:0\n" \
  "ds_read_b128 v[52:55], v74 offset:0\n" \
  "ds_read_b128 v[56:59], v74 offset:528\n" \
  "s_waitcnt lgkmcnt(2)\n" \
  "v_mfma_f32_32x32x16_bf16 v[16:31], v[48:51], v[68:71], v[76:91]\n" \
  "ds_read_b128 v[48:51], v73 offset:1024\n" \
  "v_mov_b32 v40, 0\n" \
  "v_mov_b32 v41, 0\n" \
  "v_mov_b32 v42, 0\n" \
  "v_mov_b32 v43, 0\n" \
  "v_mov_b32 v44, 0\n" \
  "v_mov_b32 v45, 0\n" \
  "v_mov_b32 v46, 0\n" \
  "v_mov_b32 v47, 0\n" \
  "v_mov_b32 v64, 0\n" \
  "v_mov_b32 v65, 0\n" \
  "v_mov_b32 v66, 0\n" \
  "v_mov_b32 v67, 0\n" \
  "s_nop 1\n" \
  "s_setprio 3\n" \
  "v_exp_f32 v16, v16\n" \
  "v_exp_f32 v17, v17\n" \
  "v_smfmac_f32_32x32x32_bf16 v[0:15], v[64:67], v[40:47], v72\n" \
  "v_exp_f32 v18, v18\n" \
  "v_exp_f32 v19, v19\n" \
  "v_exp_f32 v20, v20\n" \
  "v_exp_f32 v21, v21\n" \
  "s_waitcnt lgkmcnt(0)\n" \
  "v_mfma_f32_32x32x16_bf16 v[32:47], v[48:51], v[68:71], v[76:91]\n" \
  "ds_read_b128 v[48:51], v73 offset:2048\n" \
  "ds_read_b128 v[60:63], v74 offset:1056\n" \
  "ds_read_b128 v[64:67], v74 offset:1584\n" \
  "v_exp_f32 v22, v22\n" \
  "v_exp_f32 v23, v23\n" \
  "v_exp_f32 v24, v24\n" \
  "v_exp_f32 v25, v25\n" \
  "v_smfmac_f32_32x32x32_bf16 v[0:15], v[52:55], v[16:23], v72\n" \
  "v_exp_f32 v26, v26\n" \
  "v_exp_f32 v27, v27\n" \
  "v_exp_f32 v28, v28\n" \
  "v_exp_f32 v29, v29\n" \
  "v_exp_f32 v30, v30\n" \
  "v_exp_f32 v31, v31\n" \
  "v_exp_f32 v32, v32\n" \
  "v_exp_f32 v33, v33\n" \
  "v_smfmac_f32_32x32x32_bf16 v[0:15], v[56:59], v[24:31], v72\n" \
  "v_exp_f32 v34, v34\n" \
  "v_exp_f32 v35, v35\n" \
  "v_exp_f32 v36, v36\n" \
  "v_exp_f32 v37, v37\n" \
  "s_waitcnt lgkmcnt(0)\n" \
  "v_mfma_f32_32x32x16_bf16 v[16:31], v[48:51], v[68:71], v[76:91]\n" \
  "ds_read_b128 v[48:51], v73 offset:3072\n" \
  "ds_read_b128 v[52:55], v74 offset:2112\n" \
  "ds_read_b128 v[56:59], v74 offset:2640\n" \
  "v_exp_f32 v38, v38\n" \
  "v_exp_f32 v39, v39\n" \
  "v_exp_f32 v40, v40\n" \
  "v_exp_f32 v41, v41\n" \
  "v_smfmac_f32_32x32x32_bf16 v[0:15], v[60:63], v[32:39], v72\n" \
  "v_exp_f32 v42, v42\n" \
  "v_exp_f32 v43, v43\n" \
  "v_exp_f32 v44, v44\n" \
  "v_exp_f32 v45, v45\n" \
  "v_exp_f32 v46, v46\n" \
  "v_exp_f32 v47, v47\n" \
  "s_setprio 2\n" \
  "v_exp_f32 v16, v16\n" \
  "v_exp_f32 v17, v17\n" \
  "v_smfmac_f32_32x32x32_bf16 v[0:15], v[64:67], v[40:47], v72\n" \
  "v_exp_f32 v18, v18\n" \
  "v_exp_f32 v19, v19\n" \
  "v_exp_f32 v20, v20\n" \
  "v_exp_f32 v21, v21\n" \
  "s_waitcnt lgkmcnt(0)\n" \
  "v_mfma_f32_32x32x16_bf16 v[32:47], v[48:51], v[68:71], v[76:91]\n" \
  "ds_read_b128 v[48:51], v73 offset:4096\n" \
  "ds_read_b128 v[60:63], v74 offset:3168\n" \
  "ds_read_b128 v[64:67], v74 offset:3696\n" \
  "v_exp_f32 v22, v22\n" \
  "v_exp_f32 v23, v23\n" \
  "v_exp_f32 v24, v24\n" \
  "v_exp_f32 v25, v25\n" \
  "v_smfmac_f32_32x32x32_bf16 v[0:15], v[52:55], v[16:23], v72\n" \
  "v_exp_f32 v26, v26\n" \
  "v_exp_f32 v27, v27\n" \
  "v_exp_f32 v28, v28\n" \
  "v_exp_f32 v29, v29\n" \
  "v_exp_f32 v30, v30\n" \
  "v_exp_f32 v31, v31\n" \
  "v_exp_f32 v32, v32\n" \
  "v_exp_f32 v33, v33\n" \
  "v_smfmac_f32_32x32x32_bf16 v[0:15], v[56:59], v[24:31], v72\n" \
  "v_exp_f32 v34, v34\n" \
  "v_exp_f32 v35, v35\n" \
  "v_exp_f32 v36, v36\n" \
  "v_exp_f32 v37, v37\n" \
  "s_waitcnt lgkmcnt(0)\n" \
  "v_mfma_f32_32x32x16_bf16 v[16:31], v[48:51], v[68:71], v[76:91]\n" \
  "ds_read_b128 v[48:51], v73 offset:5120\n" \
  "ds_read_b128 v[52:55], v74 offset:4224\n" \
  "ds_read_b128 v[56:59], v74 offset:4752\n" \
  "v_exp_f32 v38, v38\n" \
  "v_exp_f32 v39, v39\n" \
  "v_exp_f32 v40, v40\n" \
  "v_exp_f32 v41, v41\n" \
  "v_smfmac_f32_32x32x32_bf16 v[0:15], v[60:63], v[32:39], v72\n" \
  "v_exp_f32 v42, v42\n" \
  "v_exp_f32 v43, v43\n" \
  "v_exp_f32 v44, v44\n" \
  "v_exp_f32 v45, v45\n" \
  "v_exp_f32 v46, v46\n" \
  "v_exp_f32 v47, v47\n" \
  "s_setprio 1\n" \
  "v_exp_f32 v16, v16\n" \
  "v_exp_f32 v17, v17\n" \
  "v_smfmac_f32_32x32x32_bf16 v[0:15], v[64:67], v[40:47], v72\n" \
  "v_exp_f32 v18, v18\n" \
  "v_exp_f32 v19, v19\n" \
  "v_exp_f32 v20, v20\n" \
  "v_exp_f32 v21, v21\n" \
  "s_waitcnt lgkmcnt(0)\n" \
  "v_mfma_f32_32x32x16_bf16 v[32:47], v[48:51], v[68:71], v[76:91]\n" \
  "ds_read_b128 v[48:51], v73 offset:6144\n" \
  "ds_read_b128 v[60:63], v74 offset:5280\n" \
  "ds_read_b128 v[64:67], v74 offset:5808\n" \
  "s_waitcnt vmcnt(0)\n" \
  "s_mov_b64 exec, 1\n" \
  "buffer_store_dwordx2 v[92:93], off, %[ws], %[fo] sc1\n" \
  "s_mov_b64 exec, -1\n" \
  "v_exp_f32 v22, v22\n" \
  "v_exp_f32 v23, v23\n" \
  "v_exp_f32 v24, v24\n" \
  "v_exp_f32 v25, v25\n" \
  "v_smfmac_f32_32x32x32_bf16 v[0:15], v[52:55], v[16:23], v72\n" \
  "v_exp_f32 v26, v26\n" \
  "v_exp_f32 v27, v27\n" \
  "v_exp_f32 v28, v28\n" \
  "v_exp_f32 v29, v29\n" \
  "v_exp_f32 v30, v30\n" \
  "v_exp_f32 v31, v31\n" \
  "v_exp_f32 v32, v32\n" \
  "v_exp_f32 v33, v33\n" \
  "v_smfmac_f32_32x32x32_bf16 v[0:15], v[56:59], v[24:31], v72\n" \
  "v_exp_f32 v34, v34\n" \
  "v_exp_f32 v35, v35\n" \
  "v_exp_f32 v36, v36\n" \
  "v_exp_f32 v37, v37\n" \
  "s_waitcnt lgkmcnt(0)\n" \
  "v_mfma_f32_32x32x16_bf16 v[16:31], v[48:51], v[68:71], v[76:91]\n" \
  "ds_read_b128 v[48:51], v73 offset:7168\n" \
  "ds_read_b128 v[52:55], v74 offset:6336\n" \
  "ds_read_b128 v[56:59], v74 offset:6864\n" \
  "v_exp_f32 v38, v38\n" \
  "v_exp_f32 v39, v39\n" \
  "v_exp_f32 v40, v40\n" \
  "v_exp_f32 v41, v41\n" \
  "v_smfmac_f32_32x32x32_bf16 v[0:15], v[60:63], v[32:39], v72\n" \
  "v_exp_f32 v42, v42\n" \
  "v_exp_f32 v43, v43\n" \
  "v_exp_f32 v44, v44\n" \
  "v_exp_f32 v45, v45\n" \
  "v_exp_f32 v46, v46\n" \
  "v_exp_f32 v47, v47\n" \
  "s_setprio 0\n" \
  "v_exp_f32 v16, v16\n" \
  "v_exp_f32 v17, v17\n" \
  "v_smfmac_f32_32x32x32_bf16 v[0:15], v[64:67], v[40:47], v72\n" \
  "v_exp_f32 v18, v18\n" \
  "v_exp_f32 v19, v19\n" \
  "v_exp_f32 v20, v20\n" \
  "v_exp_f32 v21, v21\n" \
  "s_waitcnt lgkmcnt(0)\n" \
  "v_mfma_f32_32x32x16_bf16 v[32:47], v[48:51], v[68:71], v[76:91]\n" \
  "ds_read_b128 v[60:63], v74 offset:7392\n" \
  "ds_read_b128 v[64:67], v74 offset:7920\n" \
  "buffer_load_dwordx2 v[100:101], v99, %[ws], 0 offen sc1\n" \
  "v_exp_f32 v22, v22\n" \
  "v_exp_f32 v23, v23\n" \
  "v_exp_f32 v24, v24\n" \
  "v_exp_f32 v25, v25\n" \
  "v_smfmac_f32_32x32x32_bf16 v[0:15], v[52:55], v[16:23], v72\n" \
  "v_exp_f32 v26, v26\n" \
  "v_exp_f32 v27, v27\n" \
  "v_exp_f32 v28, v28\n" \
  "v_exp_f32 v29, v29\n" \
  "v_exp_f32 v30, v30\n" \
  "v_exp_f32 v31, v31\n" \
  "v_exp_f32 v32, v32\n" \
  "v_exp_f32 v33, v33\n" \
  "v_smfmac_f32_32x32x32_bf16 v[0:15], v[56:59], v[24:31], v72\n" \
  "v_exp_f32 v34, v34\n" \
  "v_exp_f32 v35, v35\n" \
  "v_exp_f32 v36, v36\n" \
  "v_exp_f32 v37, v37\n" \
  "s_waitcnt lgkmcnt(0)\n" \
  "v_exp_f32 v38, v38\n" \
  "v_exp_f32 v39, v39\n" \
  "v_exp_f32 v40, v40\n" \
  "v_exp_f32 v41, v41\n" \
  "v_smfmac_f32_32x32x32_bf16 v[0:15], v[60:63], v[32:39], v72\n" \
  "v_exp_f32 v42, v42\n" \
  "v_exp_f32 v43, v43\n" \
  "v_exp_f32 v44, v44\n" \
  "v_exp_f32 v45, v45\n" \
  "v_exp_f32 v46, v46\n" \
  "v_exp_f32 v47, v47\n" \
  "s_nop 1\n" \
  "v_smfmac_f32_32x32x32_bf16 v[0:15], v[64:67], v[40:47], v72\n" \
  "s_nop 7\n" \
  "s_nop 7\n" \
  "s_waitcnt vmcnt(0)\n"
#define ATTN_B \
  "ds_read_b128 v[48:51], v73 offset:8192\n" \
  "ds_read_b128 v[52:55], v74 offset:8448\n" \
  "ds_read_b128 v[56:59], v74 offset:8976\n" \
  "s_waitcnt lgkmcnt(2)\n" \
  "v_mfma_f32_32x32x16_bf16 v[16:31], v[48:51], v[68:71], v[76:91]\n" \
  "ds_read_b128 v[48:51], v73 offset:9216\n" \
  "v_mov_b32 v40, 0\n" \
  "v_mov_b32 v41, 0\n" \
  "v_mov_b32 v42, 0\n" \
  "v_mov_b32 v43, 0\n" \
  "v_mov_b32 v44, 0\n" \
  "v_mov_b32 v45, 0\n" \
  "v_mov_b32 v46, 0\n" \
  "v_mov_b32 v47, 0\n" \
  "v_mov_b32 v64, 0\n" \
  "v_mov_b32 v65, 0\n" \
  "v_mov_b32 v66, 0\n" \
  "v_mov_b32 v67, 0\n" \
  "s_nop 1\n" \
  "s_setprio 2\n" \
  "v_exp_f32 v16, v16\n" \
  "v_exp_f32 v17, v17\n" \
  "v_smfmac_f32_32x32x32_bf16 v[0:15], v[64:67], v[40:47], v72\n" \
  "v_exp_f32 v18, v18\n" \
  "v_exp_f32 v19, v19\n" \
  "v_exp_f32 v20, v20\n" \
  "v_exp_f32 v21, v21\n" \
  "s_waitcnt lgkmcnt(0)\n" \
  "v_mfma_f32_32x32x16_bf16 v[32:47], v[48:51], v[68:71], v[76:91]\n" \
  "ds_read_b128 v[48:51], v73 offset:10240\n" \
  "ds_read_b128 v[60:63], v74 offset:9504\n" \
  "ds_read_b128 v[64:67], v74 offset:10032\n" \
  "s_waitcnt vmcnt(0)\n" \
  "ds_write_b128 v95, v[100:103] offset:16384\n" \
  "ds_write_b128 v96, v[104:107] offset:16896\n" \
  "s_add_u32 %[sq], %[sq], 1\n" \
  "s_and_b32 %[sq], %[sq], 7\n" \
  "s_lshl_b32 %[st], %[sq], 13\n" \
  "s_add_u32 %[st], %[st], %[skb]\n" \
  "buffer_load_dwordx4 v[108:111], v94, %[ws], %[st] offen sc1\n" \
  "s_add_u32 %[st], %[st], 0x200000\n" \
  "buffer_load_dwordx4 v[112:115], v94, %[ws], %[st] offen sc1\n" \
  "v_exp_f32 v22, v22\n" \
  "v_exp_f32 v23, v23\n" \
  "v_exp_f32 v24, v24\n" \
  "v_exp_f32 v25, v25\n" \
  "v_smfmac_f32_32x32x32_bf16 v[0:15], v[52:55], v[16:23], v72\n" \
  "v_exp_f32 v26, v26\n" \
  "v_exp_f32 v27, v27\n" \
  "v_exp_f32 v28, v28\n" \
  "v_exp_f32 v29, v29\n" \
  "v_exp_f32 v30, v30\n" \
  "v_exp_f32 v31, v31\n" \
  "v_exp_f32 v32, v32\n" \
  "v_exp_f32 v33, v33\n" \
  "v_smfmac_f32_32x32x32_bf16 v[0:15], v[56:59], v[24:31], v72\n" \
  "v_exp_f32 v34, v34\n" \
  "v_exp_f32 v35, v35\n" \
  "v_exp_f32 v36, v36\n" \
  "v_exp_f32 v37, v37\n" \
  "s_waitcnt lgkmcnt(0)\n" \
  "v_mfma_f32_32x32x16_bf16 v[16:31], v[48:51], v[68:71], v[76:91]\n" \
  "ds_read_b128 v[48:51], v73 offset:11264\n" \
  "ds_read_b128 v[52:55], v74 offset:10560\n" \
  "ds_read_b128 v[56:59], v74 offset:11088\n" \
  "v_exp_f32 v38, v38\n" \
  "v_exp_f32 v39, v39\n" \
  "v_exp_f32 v40, v40\n" \
  "v_exp_f32 v41, v41\n" \
  "v_smfmac_f32_32x32x32_bf16 v[0:15], v[60:63], v[32:39], v72\n" \
  "v_exp_f32 v42, v42\n" \
  "v_exp_f32 v43, v43\n" \
  "v_exp_f32 v44, v44\n" \
  "v_exp_f32 v45, v45\n" \
  "v_exp_f32 v46, v46\n" \
  "v_exp_f32 v47, v47\n" \
  "s_setprio 1\n" \
  "v_exp_f32 v16, v16\n" \
  "v_exp_f32 v17, v17\n" \
  "v_smfmac_f32_32x32x32_bf16 v[0:15], v[64:67], v[40:47], v72\n" \
  "v_exp_f32 v18, v18\n" \
  "v_exp_f32 v19, v19\n" \
  "v_exp_f32 v20, v20\n" \
  "v_exp_f32 v21, v21\n" \
  "s_waitcnt lgkmcnt(0)\n" \
  "v_mfma_f32_32x32x16_bf16 v[32:47], v[48:51], v[68:71], v[76:91]\n" \
  "ds_read_b128 v[48:51], v73 offset:12288\n" \
  "ds_read_b128 v[60:63], v74 offset:11616\n" \
  "ds_read_b128 v[64:67], v74 offset:12144\n" \
  "buffer_load_dwordx4 v[128:131], v97, %[rwo], %[swo] offen\n" \
  "s_add_u32 %[swo], %[swo], 16384\n" \
  "v_exp_f32 v22, v22\n" \
  "v_exp_f32 v23, v23\n" \
  "v_exp_f32 v24, v24\n" \
  "v_exp_f32 v25, v25\n" \
  "v_smfmac_f32_32x32x32_bf16 v[0:15], v[52:55], v[16:23], v72\n" \
  "v_exp_f32 v26, v26\n" \
  "v_exp_f32 v27, v27\n" \
  "v_exp_f32 v28, v28\n" \
  "v_exp_f32 v29, v29\n" \
  "v_exp_f32 v30, v30\n" \
  "v_exp_f32 v31, v31\n" \
  "v_exp_f32 v32, v32\n" \
  "v_exp_f32 v33, v33\n" \
  "v_smfmac_f32_32x32x32_bf16 v[0:15], v[56:59], v[24:31], v72\n" \
  "v_exp_f32 v34, v34\n" \
  "v_exp_f32 v35, v35\n" \
  "v_exp_f32 v36, v36\n" \
  "v_exp_f32 v37, v37\n" \
  "s_waitcnt lgkmcnt(0)\n" \
  "v_mfma_f32_32x32x16_bf16 v[16:31], v[48:51], v[68:71], v[76:91]\n" \
  "ds_read_b128 v[48:51], v73 offset:13312\n" \
  "ds_read_b128 v[52:55], v74 offset:12672\n" \
  "ds_read_b128 v[56:59], v74 offset:13200\n" \
  "v_exp_f32 v38, v38\n" \
  "v_exp_f32 v39, v39\n" \
  "v_exp_f32 v40, v40\n" \
  "v_exp_f32 v41, v41\n" \
  "v_smfmac_f32_32x32x32_bf16 v[0:15], v[60:63], v[32:39], v72\n" \
  "v_exp_f32 v42, v42\n" \
  "v_exp_f32 v43, v43\n" \
  "v_exp_f32 v44, v44\n" \
  "v_exp_f32 v45, v45\n" \
  "v_exp_f32 v46, v46\n" \
  "v_exp_f32 v47, v47\n" \
  "s_setprio 0\n" \
  "v_exp_f32 v16, v16\n" \
  "v_exp_f32 v17, v17\n" \
  "v_smfmac_f32_32x32x32_bf16 v[0:15], v[64:67], v[40:47], v72\n" \
  "v_exp_f32 v18, v18\n" \
  "v_exp_f32 v19, v19\n" \
  "v_exp_f32 v20, v20\n" \
  "v_exp_f32 v21, v21\n" \
  "s_waitcnt lgkmcnt(0)\n" \
  "v_mfma_f32_32x32x16_bf16 v[32:47], v[48:51], v[68:71], v[76:91]\n" \
  "ds_read_b128 v[48:51], v73 offset:14336\n" \
  "ds_read_b128 v[60:63], v74 offset:13728\n" \
  "ds_read_b128 v[64:67], v74 offset:14256\n" \
  "buffer_load_dwordx4 v[132:135], v97, %[rwo], %[swo] offen\n" \
  "s_add_u32 %[swo], %[swo], 16384\n" \
  "v_exp_f32 v22, v22\n" \
  "v_exp_f32 v23, v23\n" \
  "v_exp_f32 v24, v24\n" \
  "v_exp_f32 v25, v25\n" \
  "v_smfmac_f32_32x32x32_bf16 v[0:15], v[52:55], v[16:23], v72\n" \
  "v_exp_f32 v26, v26\n" \
  "v_exp_f32 v27, v27\n" \
  "v_exp_f32 v28, v28\n" \
  "v_exp_f32 v29, v29\n" \
  "v_exp_f32 v30, v30\n" \
  "v_exp_f32 v31, v31\n" \
  "v_exp_f32 v32, v32\n" \
  "v_exp_f32 v33, v33\n" \
  "v_smfmac_f32_32x32x32_bf16 v[0:15], v[56:59], v[24:31], v72\n" \
  "v_exp_f32 v34, v34\n" \
  "v_exp_f32 v35, v35\n" \
  "v_exp_f32 v36, v36\n" \
  "v_exp_f32 v37, v37\n" \
  "s_waitcnt lgkmcnt(0)\n" \
  "v_mfma_f32_32x32x16_bf16 v[16:31], v[48:51], v[68:71], v[76:91]\n" \
  "ds_read_b128 v[48:51], v73 offset:15360\n" \
  "ds_read_b128 v[52:55], v74 offset:14784\n" \
  "ds_read_b128 v[56:59], v74 offset:15312\n" \
  "s_barrier\n" \
  "v_exp_f32 v38, v38\n" \
  "v_exp_f32 v39, v39\n" \
  "v_exp_f32 v40, v40\n" \
  "v_exp_f32 v41, v41\n" \
  "v_smfmac_f32_32x32x32_bf16 v[0:15], v[60:63], v[32:39], v72\n" \
  "v_exp_f32 v42, v42\n" \
  "v_exp_f32 v43, v43\n" \
  "v_exp_f32 v44, v44\n" \
  "v_exp_f32 v45, v45\n" \
  "v_exp_f32 v46, v46\n" \
  "v_exp_f32 v47, v47\n" \
  "s_setprio 3\n" \
  "v_exp_f32 v16, v16\n" \
  "v_exp_f32 v17, v17\n" \
  "v_smfmac_f32_32x32x32_bf16 v[0:15], v[64:67], v[40:47], v72\n" \
  "v_exp_f32 v18, v18\n" \
  "v_exp_f32 v19, v19\n" \
  "v_exp_f32 v20, v20\n" \
  "v_exp_f32 v21, v21\n" \
  "s_waitcnt lgkmcnt(0)\n" \
  "v_mfma_f32_32x32x16_bf16 v[32:47], v[48:51], v[68:71], v[76:91]\n" \
  "ds_read_b128 v[48:51], v73 offset:16384\n" \
  "ds_read_b128 v[60:63], v74 offset:15840\n" \
  "ds_read_b128 v[64:67], v74 offset:16368\n" \
  "buffer_load_dwordx4 v[136:139], v97, %[rwo], %[swo] offen\n" \
  "s_add_u32 %[swo], %[swo], 16384\n" \
  "v_exp_f32 v22, v22\n" \
  "v_exp_f32 v23, v23\n" \
  "v_exp_f32 v24, v24\n" \
  "v_exp_f32 v25, v25\n" \
  "v_smfmac_f32_32x32x32_bf16 v[0:15], v[52:55], v[16:23], v72\n" \
  "v_exp_f32 v26, v26\n" \
  "v_exp_f32 v27, v27\n" \
  "v_exp_f32 v28, v28\n" \
  "v_exp_f32 v29, v29\n" \
  "v_exp_f32 v30, v30\n" \
  "v_exp_f32 v31, v31\n" \
  "v_exp_f32 v32, v32\n" \
  "v_exp_f32 v33, v33\n" \
  "v_smfmac_f32_32x32x32_bf16 v[0:15], v[56:59], v[24:31], v72\n" \
  "v_exp_f32 v34, v34\n" \
  "v_exp_f32 v35, v35\n" \
  "v_exp_f32 v36, v36\n" \
  "v_exp_f32 v37, v37\n" \
  "s_waitcnt lgkmcnt(0)\n" \
  "v_mfma_f32_32x32x16_bf16 v[16:31], v[48:51], v[68:71], v[76:91]\n" \
  "ds_read_b128 v[48:51], v73 offset:17408\n" \
  "ds_read_b128 v[52:55], v74 offset:16896\n" \
  "ds_read_b128 v[56:59], v74 offset:17424\n" \
  "v_exp_f32 v38, v38\n" \
  "v_exp_f32 v39, v39\n" \
  "v_exp_f32 v40, v40\n" \
  "v_exp_f32 v41, v41\n" \
  "v_smfmac_f32_32x32x32_bf16 v[0:15], v[60:63], v[32:39], v72\n" \
  "v_exp_f32 v42, v42\n" \
  "v_exp_f32 v43, v43\n" \
  "v_exp_f32 v44, v44\n" \
  "v_exp_f32 v45, v45\n" \
  "v_exp_f32 v46, v46\n" \
  "v_exp_f32 v47, v47\n" \
  "s_setprio 2\n" \
  "v_exp_f32 v16, v16\n" \
  "v_exp_f32 v17, v17\n" \
  "v_smfmac_f32_32x32x32_bf16 v[0:15], v[64:67], v[40:47], v72\n" \
  "v_exp_f32 v18, v18\n" \
  "v_exp_f32 v19, v19\n" \
  "v_exp_f32 v20, v20\n" \
  "v_exp_f32 v21, v21\n" \
  "s_waitcnt lgkmcnt(0)\n" \
  "v_mfma_f32_32x32x16_bf16 v[32:47], v[48:51], v[68:71], v[76:91]\n" \
  "ds_read_b128 v[48:51], v73 offset:18432\n" \
  "ds_read_b128 v[60:63], v74 offset:17952\n" \
  "ds_read_b128 v[64:67], v74 offset:18480\n" \
  "s_waitcnt vmcnt(3)\n" \
  "ds_write_b128 v95, v[108:111] offset:24576\n" \
  "ds_write_b128 v96, v[112:115] offset:25344\n" \
  "s_add_u32 %[sq], %[sq], 1\n" \
  "s_and_b32 %[sq], %[sq], 7\n" \
  "s_lshl_b32 %[st], %[sq], 13\n" \
  "s_add_u32 %[st], %[st], %[skb]\n" \
  "buffer_load_dwordx4 v[100:103], v94, %[ws], %[st] offen sc1\n" \
  "s_add_u32 %[st], %[st], 0x200000\n" \
  "buffer_load_dwordx4 v[104:107], v94, %[ws], %[st] offen sc1\n" \
  "v_exp_f32 v22, v22\n" \
  "v_exp_f32 v23, v23\n" \
  "v_exp_f32 v24, v24\n" \
  "v_exp_f32 v25, v25\n" \
  "v_smfmac_f32_32x32x32_bf16 v[0:15], v[52:55], v[16:23], v72\n" \
  "v_exp_f32 v26, v26\n" \
  "v_exp_f32 v27, v27\n" \
  "v_exp_f32 v28, v28\n" \
  "v_exp_f32 v29, v29\n" \
  "v_exp_f32 v30, v30\n" \
  "v_exp_f32 v31, v31\n" \
  "v_exp_f32 v32, v32\n" \
  "v_exp_f32 v33, v33\n" \
  "v_smfmac_f32_32x32x32_bf16 v[0:15], v[56:59], v[24:31], v72\n" \
  "v_exp_f32 v34, v34\n" \
  "v_exp_f32 v35, v35\n" \
  "v_exp_f32 v36, v36\n" \
  "v_exp_f32 v37, v37\n" \
  "s_waitcnt lgkmcnt(0)\n" \
  "v_mfma_f32_32x32x16_bf16 v[16:31], v[48:51], v[68:71], v[76:91]\n" \
  "ds_read_b128 v[48:51], v73 offset:19456\n" \
  "ds_read_b128 v[52:55], v74 offset:19008\n" \
  "ds_read_b128 v[56:59], v74 offset:19536\n" \
  "buffer_load_dwordx4 v[140:143], v97, %[rwo], %[swo] offen\n" \
  "s_add_u32 %[swo], %[swo], 16384\n" \
  "v_exp_f32 v38, v38\n" \
  "v_exp_f32 v39, v39\n" \
  "v_exp_f32 v40, v40\n" \
  "v_exp_f32 v41, v41\n" \
  "v_smfmac_f32_32x32x32_bf16 v[0:15], v[60:63], v[32:39], v72\n" \
  "v_exp_f32 v42, v42\n" \
  "v_exp_f32 v43, v43\n" \
  "v_exp_f32 v44, v44\n" \
  "v_exp_f32 v45, v45\n" \
  "v_exp_f32 v46, v46\n" \
  "v_exp_f32 v47, v47\n" \
  "s_setprio 1\n" \
  "v_exp_f32 v16, v16\n" \
  "v_exp_f32 v17, v17\n" \
  "v_smfmac_f32_32x32x32_bf16 v[0:15], v[64:67], v[40:47], v72\n" \
  "v_exp_f32 v18, v18\n" \
  "v_exp_f32 v19, v19\n" \
  "v_exp_f32 v20, v20\n" \
  "v_exp_f32 v21, v21\n" \
  "s_waitcnt lgkmcnt(0)\n" \
  "v_mfma_f32_32x32x16_bf16 v[32:47], v[48:51], v[68:71], v[76:91]\n" \
  "ds_read_b128 v[48:51], v73 offset:20480\n" \
  "ds_read_b128 v[60:63], v74 offset:20064\n" \
  "ds_read_b128 v[64:67], v74 offset:20592\n" \
  "buffer_load_dwordx4 v[144:147], v97, %[rwo], %[swo] offen\n" \
  "s_add_u32 %[swo], %[swo], 16384\n" \
  "v_exp_f32 v22, v22\n" \
  "v_exp_f32 v23, v23\n" \
  "v_exp_f32 v24, v24\n" \
  "v_exp_f32 v25, v25\n" \
  "v_smfmac_f32_32x32x32_bf16 v[0:15], v[52:55], v[16:23], v72\n" \
  "v_exp_f32 v26, v26\n" \
  "v_exp_f32 v27, v27\n" \
  "v_exp_f32 v28, v28\n" \
  "v_exp_f32 v29, v29\n" \
  "v_exp_f32 v30, v30\n" \
  "v_exp_f32 v31, v31\n" \
  "v_exp_f32 v32, v32\n" \
  "v_exp_f32 v33, v33\n" \
  "v_smfmac_f32_32x32x32_bf16 v[0:15], v[56:59], v[24:31], v72\n" \
  "v_exp_f32 v34, v34\n" \
  "v_exp_f32 v35, v35\n" \
  "v_exp_f32 v36, v36\n" \
  "v_exp_f32 v37, v37\n" \
  "s_waitcnt lgkmcnt(0)\n" \
  "v_mfma_f32_32x32x16_bf16 v[16:31], v[48:51], v[68:71], v[76:91]\n" \
  "ds_read_b128 v[48:51], v73 offset:21504\n" \
  "ds_read_b128 v[52:55], v74 offset:21120\n" \
  "ds_read_b128 v[56:59], v74 offset:21648\n" \
  "buffer_load_dwordx4 v[148:151], v97, %[rwo], %[swo] offen\n" \
  "s_add_u32 %[swo], %[swo], 16384\n" \
  "v_exp_f32 v38, v38\n" \
  "v_exp_f32 v39, v39\n" \
  "v_exp_f32 v40, v40\n" \
  "v_exp_f32 v41, v41\n" \
  "v_smfmac_f32_32x32x32_bf16 v[0:15], v[60:63], v[32:39], v72\n" \
  "v_exp_f32 v42, v42\n" \
  "v_exp_f32 v43, v43\n" \
  "v_exp_f32 v44, v44\n" \
  "v_exp_f32 v45, v45\n" \
  "v_exp_f32 v46, v46\n" \
  "v_exp_f32 v47, v47\n" \
  "s_setprio 0\n" \
  "v_exp_f32 v16, v16\n" \
  "v_exp_f32 v17, v17\n" \
  "v_smfmac_f32_32x32x32_bf16 v[0:15], v[64:67], v[40:47], v72\n" \
  "v_exp_f32 v18, v18\n" \
  "v_exp_f32 v19, v19\n" \
  "v_exp_f32 v20, v20\n" \
  "v_exp_f32 v21, v21\n" \
  "s_waitcnt lgkmcnt(0)\n" \
  "v_mfma_f32_32x32x16_bf16 v[32:47], v[48:51], v[68:71], v[76:91]\n" \
  "ds_read_b128 v[48:51], v73 offset:22528\n" \
  "ds_read_b128 v[60:63], v74 offset:22176\n" \
  "ds_read_b128 v[64:67], v74 offset:22704\n" \
  "buffer_load_dwordx4 v[152:155], v97, %[rwo], %[swo] offen\n" \
  "s_add_u32 %[swo], %[swo], 16384\n" \
  "v_exp_f32 v22, v22\n" \
  "v_exp_f32 v23, v23\n" \
  "v_exp_f32 v24, v24\n" \
  "v_exp_f32 v25, v25\n" \
  "v_smfmac_f32_32x32x32_bf16 v[0:15], v[52:55], v[16:23], v72\n" \
  "v_exp_f32 v26, v26\n" \
  "v_exp_f32 v27, v27\n" \
  "v_exp_f32 v28, v28\n" \
  "v_exp_f32 v29, v29\n" \
  "v_exp_f32 v30, v30\n" \
  "v_exp_f32 v31, v31\n" \
  "v_exp_f32 v32, v32\n" \
  "v_exp_f32 v33, v33\n" \
  "v_smfmac_f32_32x32x32_bf16 v[0:15], v[56:59], v[24:31], v72\n" \
  "v_exp_f32 v34, v34\n" \
  "v_exp_f32 v35, v35\n" \
  "v_exp_f32 v36, v36\n" \
  "v_exp_f32 v37, v37\n" \
  "s_waitcnt lgkmcnt(0)\n" \
  "v_mfma_f32_32x32x16_bf16 v[16:31], v[48:51], v[68:71], v[76:91]\n" \
  "ds_read_b128 v[48:51], v73 offset:23552\n" \
  "ds_read_b128 v[52:55], v74 offset:23232\n" \
  "ds_read_b128 v[56:59], v74 offset:23760\n" \
  "s_barrier\n" \
  "v_exp_f32 v38, v38\n" \
  "v_exp_f32 v39, v39\n" \
  "v_exp_f32 v40, v40\n" \
  "v_exp_f32 v41, v41\n" \
  "v_smfmac_f32_32x32x32_bf16 v[0:15], v[60:63], v[32:39], v72\n" \
  "v_exp_f32 v42, v42\n" \
  "v_exp_f32 v43, v43\n" \
  "v_exp_f32 v44, v44\n" \
  "v_exp_f32 v45, v45\n" \
  "v_exp_f32 v46, v46\n" \
  "v_exp_f32 v47, v47\n" \
  "s_setprio 3\n" \
  "v_exp_f32 v16, v16\n" \
  "v_exp_f32 v17, v17\n" \
  "v_smfmac_f32_32x32x32_bf16 v[0:15], v[64:67], v[40:47], v72\n" \
  "v_exp_f32 v18, v18\n" \
  "v_exp_f32 v19, v19\n" \
  "v_exp_f32 v20, v20\n" \
  "v_exp_f32 v21, v21\n" \
  "s_waitcnt lgkmcnt(0)\n" \
  "v_mfma_f32_32x32x16_bf16 v[32:47], v[48:51], v[68:71], v[76:91]\n" \
  "ds_read_b128 v[48:51], v73 offset:24576\n" \
  "ds_read_b128 v[60:63], v74 offset:24288\n" \
  "ds_read_b128 v[64:67], v74 offset:24816\n" \
  "buffer_load_dwordx4 v[156:159], v97, %[rwo], %[swo] offen\n" \
  "s_add_u32 %[swo], %[swo], 16384\n" \
  "v_exp_f32 v22, v22\n" \
  "v_exp_f32 v23, v23\n" \
  "v_exp_f32 v24, v24\n" \
  "v_exp_f32 v25, v25\n" \
  "v_smfmac_f32_32x32x32_bf16 v[0:15], v[52:55], v[16:23], v72\n" \
  "v_exp_f32 v26, v26\n" \
  "v_exp_f32 v27, v27\n" \
  "v_exp_f32 v28, v28\n" \
  "v_exp_f32 v29, v29\n" \
  "v_exp_f32 v30, v30\n" \
  "v_exp_f32 v31, v31\n" \
  "v_exp_f32 v32, v32\n" \
  "v_exp_f32 v33, v33\n" \
  "v_smfmac_f32_32x32x32_bf16 v[0:15], v[56:59], v[24:31], v72\n" \
  "v_exp_f32 v34, v34\n" \
  "v_exp_f32 v35, v35\n" \
  "v_exp_f32 v36, v36\n" \
  "v_exp_f32 v37, v37\n" \
  "s_waitcnt lgkmcnt(0)\n" \
  "v_mfma_f32_32x32x16_bf16 v[16:31], v[48:51], v[68:71], v[76:91]\n" \
  "ds_read_b128 v[48:51], v73 offset:25600\n" \
  "ds_read_b128 v[52:55], v74 offset:25344\n" \
  "ds_read_b128 v[56:59], v74 offset:25872\n" \
  "v_exp_f32 v38, v38\n" \
  "v_exp_f32 v39, v39\n" \
  "v_exp_f32 v40, v40\n" \
  "v_exp_f32 v41, v41\n" \
  "v_smfmac_f32_32x32x32_bf16 v[0:15], v[60:63], v[32:39], v72\n" \
  "v_exp_f32 v42, v42\n" \
  "v_exp_f32 v43, v43\n" \
  "v_exp_f32 v44, v44\n" \
  "v_exp_f32 v45, v45\n" \
  "v_exp_f32 v46, v46\n" \
  "v_exp_f32 v47, v47\n" \
  "s_setprio 2\n" \
  "v_exp_f32 v16, v16\n" \
  "v_exp_f32 v17, v17\n" \
  "v_smfmac_f32_32x32x32_bf16 v[0:15], v[64:67], v[40:47], v72\n" \
  "v_exp_f32 v18, v18\n" \
  "v_exp_f32 v19, v19\n" \
  "v_exp_f32 v20, v20\n" \
  "v_exp_f32 v21, v21\n" \
  "s_waitcnt lgkmcnt(0)\n" \
  "v_mfma_f32_32x32x16_bf16 v[32:47], v[48:51], v[68:71], v[76:91]\n" \
  "ds_read_b128 v[48:51], v73 offset:26624\n" \
  "ds_read_b128 v[60:63], v74 offset:26400\n" \
  "ds_read_b128 v[64:67], v74 offset:26928\n" \
  "s_waitcnt vmcnt(5)\n" \
  "ds_write_b128 v95, v[100:103] offset:32768\n" \
  "ds_write_b128 v96, v[104:107] offset:33792\n" \
  "s_add_u32 %[sq], %[sq], 1\n" \
  "s_and_b32 %[sq], %[sq], 7\n" \
  "s_lshl_b32 %[st], %[sq], 13\n" \
  "s_add_u32 %[st], %[st], %[skb]\n" \
  "buffer_load_dwordx4 v[108:111], v94, %[ws], %[st] offen sc1\n" \
  "s_add_u32 %[st], %[st], 0x200000\n" \
  "buffer_load_dwordx4 v[112:115], v94, %[ws], %[st] offen sc1\n" \
  "v_exp_f32 v22, v22\n" \
  "v_exp_f32 v23, v23\n" \
  "v_exp_f32 v24, v24\n" \
  "v_exp_f32 v25, v25\n" \
  "v_smfmac_f32_32x32x32_bf16 v[0:15], v[52:55], v[16:23], v72\n" \
  "v_exp_f32 v26, v26\n" \
  "v_exp_f32 v27, v27\n" \
  "v_exp_f32 v28, v28\n" \
  "v_exp_f32 v29, v29\n" \
  "v_exp_f32 v30, v30\n" \
  "v_exp_f32 v31, v31\n" \
  "v_exp_f32 v32, v32\n" \
  "v_exp_f32 v33, v33\n" \
  "v_smfmac_f32_32x32x32_bf16 v[0:15], v[56:59], v[24:31], v72\n" \
  "v_exp_f32 v34, v34\n" \
  "v_exp_f32 v35, v35\n" \
  "v_exp_f32 v36, v36\n" \
  "v_exp_f32 v37, v37\n" \
  "s_waitcnt lgkmcnt(0)\n" \
  "v_mfma_f32_32x32x16_bf16 v[16:31], v[48:51], v[68:71], v[76:91]\n" \
  "ds_read_b128 v[48:51], v73 offset:27648\n" \
  "ds_read_b128 v[52:55], v74 offset:27456\n" \
  "ds_read_b128 v[56:59], v74 offset:27984\n" \
  "buffer_load_dwordx4 v[160:163], v97, %[rwo], %[swo] offen\n" \
  "s_add_u32 %[swo], %[swo], 16384\n" \
  "v_exp_f32 v38, v38\n" \
  "v_exp_f32 v39, v39\n" \
  "v_exp_f32 v40, v40\n" \
  "v_exp_f32 v41, v41\n" \
  "v_smfmac_f32_32x32x32_bf16 v[0:15], v[60:63], v[32:39], v72\n" \
  "v_exp_f32 v42, v42\n" \
  "v_exp_f32 v43, v43\n" \
  "v_exp_f32 v44, v44\n" \
  "v_exp_f32 v45, v45\n" \
  "v_exp_f32 v46, v46\n" \
  "v_exp_f32 v47, v47\n" \
  "s_setprio 1\n" \
  "v_exp_f32 v16, v16\n" \
  "v_exp_f32 v17, v17\n" \
  "v_smfmac_f32_32x32x32_bf16 v[0:15], v[64:67], v[40:47], v72\n" \
  "v_exp_f32 v18, v18\n" \
  "v_exp_f32 v19, v19\n" \
  "v_exp_f32 v20, v20\n" \
  "v_exp_f32 v21, v21\n" \
  "s_waitcnt lgkmcnt(0)\n" \
  "v_mfma_f32_32x32x16_bf16 v[32:47], v[48:51], v[68:71], v[76:91]\n" \
  "ds_read_b128 v[48:51], v73 offset:28672\n" \
  "ds_read_b128 v[60:63], v74 offset:28512\n" \
  "ds_read_b128 v[64:67], v74 offset:29040\n" \
  "buffer_load_dwordx4 v[164:167], v97, %[rwo], %[swo] offen\n" \
  "s_add_u32 %[swo], %[swo], 16384\n" \
  "v_exp_f32 v22, v22\n" \
  "v_exp_f32 v23, v23\n" \
  "v_exp_f32 v24, v24\n" \
  "v_exp_f32 v25, v25\n" \
  "v_smfmac_f32_32x32x32_bf16 v[0:15], v[52:55], v[16:23], v72\n" \
  "v_exp_f32 v26, v26\n" \
  "v_exp_f32 v27, v27\n" \
  "v_exp_f32 v28, v28\n" \
  "v_exp_f32 v29, v29\n" \
  "v_exp_f32 v30, v30\n" \
  "v_exp_f32 v31, v31\n" \
  "v_exp_f32 v32, v32\n" \
  "v_exp_f32 v33, v33\n" \
  "v_smfmac_f32_32x32x32_bf16 v[0:15], v[56:59], v[24:31], v72\n" \
  "v_exp_f32 v34, v34\n" \
  "v_exp_f32 v35, v35\n" \
  "v_exp_f32 v36, v36\n" \
  "v_exp_f32 v37, v37\n" \
  "s_waitcnt lgkmcnt(0)\n" \
  "v_mfma_f32_32x32x16_bf16 v[16:31], v[48:51], v[68:71], v[76:91]\n" \
  "ds_read_b128 v[48:51], v73 offset:29696\n" \
  "ds_read_b128 v[52:55], v74 offset:29568\n" \
  "ds_read_b128 v[56:59], v74 offset:30096\n" \
  "buffer_load_dwordx4 v[168:171], v97, %[rwo], %[swo] offen\n" \
  "s_add_u32 %[swo], %[swo], 16384\n" \
  "v_exp_f32 v38, v38\n" \
  "v_exp_f32 v39, v39\n" \
  "v_exp_f32 v40, v40\n" \
  "v_exp_f32 v41, v41\n" \
  "v_smfmac_f32_32x32x32_bf16 v[0:15], v[60:63], v[32:39], v72\n" \
  "v_exp_f32 v42, v42\n" \
  "v_exp_f32 v43, v43\n" \
  "v_exp_f32 v44, v44\n" \
  "v_exp_f32 v45, v45\n" \
  "v_exp_f32 v46, v46\n" \
  "v_exp_f32 v47, v47\n" \
  "s_setprio 0\n" \
  "v_exp_f32 v16, v16\n" \
  "v_exp_f32 v17, v17\n" \
  "v_smfmac_f32_32x32x32_bf16 v[0:15], v[64:67], v[40:47], v72\n" \
  "v_exp_f32 v18, v18\n" \
  "v_exp_f32 v19, v19\n" \
  "v_exp_f32 v20, v20\n" \
  "v_exp_f32 v21, v21\n" \
  "s_waitcnt lgkmcnt(0)\n" \
  "v_mfma_f32_32x32x16_bf16 v[32:47], v[48:51], v[68:71], v[76:91]\n" \
  "ds_read_b128 v[48:51], v73 offset:30720\n" \
  "ds_read_b128 v[60:63], v74 offset:30624\n" \
  "ds_read_b128 v[64:67], v74 offset:31152\n" \
  "buffer_load_dwordx4 v[172:175], v97, %[rwo], %[swo] offen\n" \
  "s_add_u32 %[swo], %[swo], 16384\n" \
  "v_exp_f32 v22, v22\n" \
  "v_exp_f32 v23, v23\n" \
  "v_exp_f32 v24, v24\n" \
  "v_exp_f32 v25, v25\n" \
  "v_smfmac_f32_32x32x32_bf16 v[0:15], v[52:55], v[16:23], v72\n" \
  "v_exp_f32 v26, v26\n" \
  "v_exp_f32 v27, v27\n" \
  "v_exp_f32 v28, v28\n" \
  "v_exp_f32 v29, v29\n" \
  "v_exp_f32 v30, v30\n" \
  "v_exp_f32 v31, v31\n" \
  "v_exp_f32 v32, v32\n" \
  "v_exp_f32 v33, v33\n" \
  "v_smfmac_f32_32x32x32_bf16 v[0:15], v[56:59], v[24:31], v72\n" \
  "v_exp_f32 v34, v34\n" \
  "v_exp_f32 v35, v35\n" \
  "v_exp_f32 v36, v36\n" \
  "v_exp_f32 v37, v37\n" \
  "s_waitcnt lgkmcnt(0)\n" \
  "v_mfma_f32_32x32x16_bf16 v[16:31], v[48:51], v[68:71], v[76:91]\n" \
  "ds_read_b128 v[48:51], v73 offset:31744\n" \
  "ds_read_b128 v[52:55], v74 offset:31680\n" \
  "ds_read_b128 v[56:59], v74 offset:32208\n" \
  "s_barrier\n" \
  "v_exp_f32 v38, v38\n" \
  "v_exp_f32 v39, v39\n" \
  "v_exp_f32 v40, v40\n" \
  "v_exp_f32 v41, v41\n" \
  "v_smfmac_f32_32x32x32_bf16 v[0:15], v[60:63], v[32:39], v72\n" \
  "v_exp_f32 v42, v42\n" \
  "v_exp_f32 v43, v43\n" \
  "v_exp_f32 v44, v44\n" \
  "v_exp_f32 v45, v45\n" \
  "v_exp_f32 v46, v46\n" \
  "v_exp_f32 v47, v47\n" \
  "s_setprio 3\n" \
  "v_exp_f32 v16, v16\n" \
  "v_exp_f32 v17, v17\n" \
  "v_smfmac_f32_32x32x32_bf16 v[0:15], v[64:67], v[40:47], v72\n" \
  "v_exp_f32 v18, v18\n" \
  "v_exp_f32 v19, v19\n" \
  "v_exp_f32 v20, v20\n" \
  "v_exp_f32 v21, v21\n" \
  "s_waitcnt lgkmcnt(0)\n" \
  "v_mfma_f32_32x32x16_bf16 v[32:47], v[48:51], v[68:71], v[76:91]\n" \
  "ds_read_b128 v[48:51], v73 offset:32768\n" \
  "ds_read_b128 v[60:63], v74 offset:32736\n" \
  "ds_read_b128 v[64:67], v74 offset:33264\n" \
  "buffer_load_dwordx4 v[176:179], v97, %[rwo], %[swo] offen\n" \
  "s_add_u32 %[swo], %[swo], 16384\n" \
  "v_exp_f32 v22, v22\n" \
  "v_exp_f32 v23, v23\n" \
  "v_exp_f32 v24, v24\n" \
  "v_exp_f32 v25, v25\n" \
  "v_smfmac_f32_32x32x32_bf16 v[0:15], v[52:55], v[16:23], v72\n" \
  "v_exp_f32 v26, v26\n" \
  "v_exp_f32 v27, v27\n" \
  "v_exp_f32 v28, v28\n" \
  "v_exp_f32 v29, v29\n" \
  "v_exp_f32 v30, v30\n" \
  "v_exp_f32 v31, v31\n" \
  "v_exp_f32 v32, v32\n" \
  "v_exp_f32 v33, v33\n" \
  "v_smfmac_f32_32x32x32_bf16 v[0:15], v[56:59], v[24:31], v72\n" \
  "v_exp_f32 v34, v34\n" \
  "v_exp_f32 v35, v35\n" \
  "v_exp_f32 v36, v36\n" \
  "v_exp_f32 v37, v37\n" \
  "s_waitcnt lgkmcnt(0)\n" \
  "v_mfma_f32_32x32x16_bf16 v[16:31], v[48:51], v[68:71], v[76:91]\n" \
  "ds_read_b128 v[48:51], v73 offset:33792\n" \
  "ds_read_b128 v[52:55], v75 offset:0\n" \
  "ds_read_b128 v[56:59], v75 offset:528\n" \
  "v_exp_f32 v38, v38\n" \
  "v_exp_f32 v39, v39\n" \
  "v_exp_f32 v40, v40\n" \
  "v_exp_f32 v41, v41\n" \
  "v_smfmac_f32_32x32x32_bf16 v[0:15], v[60:63], v[32:39], v72\n" \
  "v_exp_f32 v42, v42\n" \
  "v_exp_f32 v43, v43\n" \
  "v_exp_f32 v44, v44\n" \
  "v_exp_f32 v45, v45\n" \
  "v_exp_f32 v46, v46\n" \
  "v_exp_f32 v47, v47\n" \
  "s_setprio 2\n" \
  "v_exp_f32 v16, v16\n" \
  "v_exp_f32 v17, v17\n" \
  "v_smfmac_f32_32x32x32_bf16 v[0:15], v[64:67], v[40:47], v72\n" \
  "v_exp_f32 v18, v18\n" \
  "v_exp_f32 v19, v19\n" \
  "v_exp_f32 v20, v20\n" \
  "v_exp_f32 v21, v21\n" \
  "s_waitcnt lgkmcnt(0)\n" \
  "v_mfma_f32_32x32x16_bf16 v[32:47], v[48:51], v[68:71], v[76:91]\n" \
  "ds_read_b128 v[48:51], v73 offset:34816\n" \
  "ds_read_b128 v[60:63], v75 offset:1056\n" \
  "ds_read_b128 v[64:67], v75 offset:1584\n" \
  "s_waitcnt vmcnt(5)\n" \
  "ds_write_b128 v95, v[108:111] offset:40960\n" \
  "ds_write_b128 v96, v[112:115] offset:42240\n" \
  "s_add_u32 %[sq], %[sq], 1\n" \
  "s_and_b32 %[sq], %[sq], 7\n" \
  "s_lshl_b32 %[st], %[sq], 13\n" \
  "s_add_u32 %[st], %[st], %[skb]\n" \
  "buffer_load_dwordx4 v[100:103], v94, %[ws], %[st] offen sc1\n" \
  "s_add_u32 %[st], %[st], 0x200000\n" \
  "buffer_load_dwordx4 v[104:107], v94, %[ws], %[st] offen sc1\n" \
  "v_exp_f32 v22, v22\n" \
  "v_exp_f32 v23, v23\n" \
  "v_exp_f32 v24, v24\n" \
  "v_exp_f32 v25, v25\n" \
  "v_smfmac_f32_32x32x32_bf16 v[0:15], v[52:55], v[16:23], v72\n" \
  "v_exp_f32 v26, v26\n" \
  "v_exp_f32 v27, v27\n" \
  "v_exp_f32 v28, v28\n" \
  "v_exp_f32 v29, v29\n" \
  "v_exp_f32 v30, v30\n" \
  "v_exp_f32 v31, v31\n" \
  "v_exp_f32 v32, v32\n" \
  "v_exp_f32 v33, v33\n" \
  "v_smfmac_f32_32x32x32_bf16 v[0:15], v[56:59], v[24:31], v72\n" \
  "v_exp_f32 v34, v34\n" \
  "v_exp_f32 v35, v35\n" \
  "v_exp_f32 v36, v36\n" \
  "v_exp_f32 v37, v37\n" \
  "s_waitcnt lgkmcnt(0)\n" \
  "v_mfma_f32_32x32x16_bf16 v[16:31], v[48:51], v[68:71], v[76:91]\n" \
  "ds_read_b128 v[48:51], v73 offset:35840\n" \
  "ds_read_b128 v[52:55], v75 offset:2112\n" \
  "ds_read_b128 v[56:59], v75 offset:2640\n" \
  "buffer_load_dwordx4 v[180:183], v97, %[rwo], %[swo] offen\n" \
  "s_add_u32 %[swo], %[swo], 16384\n" \
  "v_exp_f32 v38, v38\n" \
  "v_exp_f32 v39, v39\n" \
  "v_exp_f32 v40, v40\n" \
  "v_exp_f32 v41, v41\n" \
  "v_smfmac_f32_32x32x32_bf16 v[0:15], v[60:63], v[32:39], v72\n" \
  "v_exp_f32 v42, v42\n" \
  "v_exp_f32 v43, v43\n" \
  "v_exp_f32 v44, v44\n" \
  "v_exp_f32 v45, v45\n" \
  "v_exp_f32 v46, v46\n" \
  "v_exp_f32 v47, v47\n" \
  "s_setprio 1\n" \
  "v_exp_f32 v16, v16\n" \
  "v_exp_f32 v17, v17\n" \
  "v_smfmac_f32_32x32x32_bf16 v[0:15], v[64:67], v[40:47], v72\n" \
  "v_exp_f32 v18, v18\n" \
  "v_exp_f32 v19, v19\n" \
  "v_exp_f32 v20, v20\n" \
  "v_exp_f32 v21, v21\n" \
  "s_waitcnt lgkmcnt(0)\n" \
  "v_mfma_f32_32x32x16_bf16 v[32:47], v[48:51], v[68:71], v[76:91]\n" \
  "ds_read_b128 v[48:51], v73 offset:36864\n" \
  "ds_read_b128 v[60:63], v75 offset:3168\n" \
  "ds_read_b128 v[64:67], v75 offset:3696\n" \
  "buffer_load_dwordx4 v[184:187], v97, %[rwo], %[swo] offen\n" \
  "s_add_u32 %[swo], %[swo], 16384\n" \
  "v_exp_f32 v22, v22\n" \
  "v_exp_f32 v23, v23\n" \
  "v_exp_f32 v24, v24\n" \
  "v_exp_f32 v25, v25\n" \
  "v_smfmac_f32_32x32x32_bf16 v[0:15], v[52:55], v[16:23], v72\n" \
  "v_exp_f32 v26, v26\n" \
  "v_exp_f32 v27, v27\n" \
  "v_exp_f32 v28, v28\n" \
  "v_exp_f32 v29, v29\n" \
  "v_exp_f32 v30, v30\n" \
  "v_exp_f32 v31, v31\n" \
  "v_exp_f32 v32, v32\n" \
  "v_exp_f32 v33, v33\n" \
  "v_smfmac_f32_32x32x32_bf16 v[0:15], v[56:59], v[24:31], v72\n" \
  "v_exp_f32 v34, v34\n" \
  "v_exp_f32 v35, v35\n" \
  "v_exp_f32 v36, v36\n" \
  "v_exp_f32 v37, v37\n" \
  "s_waitcnt lgkmcnt(0)\n" \
  "v_mfma_f32_32x32x16_bf16 v[16:31], v[48:51], v[68:71], v[76:91]\n" \
  "ds_read_b128 v[48:51], v73 offset:37888\n" \
  "ds_read_b128 v[52:55], v75 offset:4224\n" \
  "ds_read_b128 v[56:59], v75 offset:4752\n" \
  "buffer_load_dwordx4 v[188:191], v97, %[rwo], %[swo] offen\n" \
  "s_add_u32 %[swo], %[swo], 16384\n" \
  "v_exp_f32 v38, v38\n" \
  "v_exp_f32 v39, v39\n" \
  "v_exp_f32 v40, v40\n" \
  "v_exp_f32 v41, v41\n" \
  "v_smfmac_f32_32x32x32_bf16 v[0:15], v[60:63], v[32:39], v72\n" \
  "v_exp_f32 v42, v42\n" \
  "v_exp_f32 v43, v43\n" \
  "v_exp_f32 v44, v44\n" \
  "v_exp_f32 v45, v45\n" \
  "v_exp_f32 v46, v46\n" \
  "v_exp_f32 v47, v47\n" \
  "s_setprio 0\n" \
  "v_exp_f32 v16, v16\n" \
  "v_exp_f32 v17, v17\n" \
  "v_smfmac_f32_32x32x32_bf16 v[0:15], v[64:67], v[40:47], v72\n" \
  "v_exp_f32 v18, v18\n" \
  "v_exp_f32 v19, v19\n" \
  "v_exp_f32 v20, v20\n" \
  "v_exp_f32 v21, v21\n" \
  "s_waitcnt lgkmcnt(0)\n" \
  "v_mfma_f32_32x32x16_bf16 v[32:47], v[48:51], v[68:71], v[76:91]\n" \
  "ds_read_b128 v[48:51], v73 offset:38912\n" \
  "ds_read_b128 v[60:63], v75 offset:5280\n" \
  "ds_read_b128 v[64:67], v75 offset:5808\n" \
  "buffer_load_dwordx4 v[192:195], v98, %[refa], %[sef] offen\n" \
  "s_add_u32 %[sef], %[sef], %[sefs]\n" \
  "v_exp_f32 v22, v22\n" \
  "v_exp_f32 v23, v23\n" \
  "v_exp_f32 v24, v24\n" \
  "v_exp_f32 v25, v25\n" \
  "v_smfmac_f32_32x32x32_bf16 v[0:15], v[52:55], v[16:23], v72\n" \
  "v_exp_f32 v26, v26\n" \
  "v_exp_f32 v27, v27\n" \
  "v_exp_f32 v28, v28\n" \
  "v_exp_f32 v29, v29\n" \
  "v_exp_f32 v30, v30\n" \
  "v_exp_f32 v31, v31\n" \
  "v_exp_f32 v32, v32\n" \
  "v_exp_f32 v33, v33\n" \
  "v_smfmac_f32_32x32x32_bf16 v[0:15], v[56:59], v[24:31], v72\n" \
  "v_exp_f32 v34, v34\n" \
  "v_exp_f32 v35, v35\n" \
  "v_exp_f32 v36, v36\n" \
  "v_exp_f32 v37, v37\n" \
  "s_waitcnt lgkmcnt(0)\n" \
  "v_mfma_f32_32x32x16_bf16 v[16:31], v[48:51], v[68:71], v[76:91]\n" \
  "ds_read_b128 v[48:51], v73 offset:39936\n" \
  "ds_read_b128 v[52:55], v75 offset:6336\n" \
  "ds_read_b128 v[56:59], v75 offset:6864\n" \
  "s_barrier\n" \
  "v_exp_f32 v38, v38\n" \
  "v_exp_f32 v39, v39\n" \
  "v_exp_f32 v40, v40\n" \
  "v_exp_f32 v41, v41\n" \
  "v_smfmac_f32_32x32x32_bf16 v[0:15], v[60:63], v[32:39], v72\n" \
  "v_exp_f32 v42, v42\n" \
  "v_exp_f32 v43, v43\n" \
  "v_exp_f32 v44, v44\n" \
  "v_exp_f32 v45, v45\n" \
  "v_exp_f32 v46, v46\n" \
  "v_exp_f32 v47, v47\n" \
  "s_setprio 3\n" \
  "v_exp_f32 v16, v16\n" \
  "v_exp_f32 v17, v17\n" \
  "v_smfmac_f32_32x32x32_bf16 v[0:15], v[64:67], v[40:47], v72\n" \
  "v_exp_f32 v18, v18\n" \
  "v_exp_f32 v19, v19\n" \
  "v_exp_f32 v20, v20\n" \
  "v_exp_f32 v21, v21\n" \
  "s_waitcnt lgkmcnt(0)\n" \
  "v_mfma_f32_32x32x16_bf16 v[32:47], v[48:51], v[68:71], v[76:91]\n" \
  "ds_read_b128 v[48:51], v73 offset:40960\n" \
  "ds_read_b128 v[60:63], v75 offset:7392\n" \
  "ds_read_b128 v[64:67], v75 offset:7920\n" \
  "buffer_load_dwordx4 v[196:199], v98, %[refa], %[sef] offen\n" \
  "s_add_u32 %[sef], %[sef], %[sefs]\n" \
  "s_sub_u32 %[sef], %[sef], %[sefr]\n" \
  "v_exp_f32 v22, v22\n" \
  "v_exp_f32 v23, v23\n" \
  "v_exp_f32 v24, v24\n" \
  "v_exp_f32 v25, v25\n" \
  "v_smfmac_f32_32x32x32_bf16 v[0:15], v[52:55], v[16:23], v72\n" \
  "v_exp_f32 v26, v26\n" \
  "v_exp_f32 v27, v27\n" \
  "v_exp_f32 v28, v28\n" \
  "v_exp_f32 v29, v29\n" \
  "v_exp_f32 v30, v30\n" \
  "v_exp_f32 v31, v31\n" \
  "v_exp_f32 v32, v32\n" \
  "v_exp_f32 v33, v33\n" \
  "v_smfmac_f32_32x32x32_bf16 v[0:15], v[56:59], v[24:31], v72\n" \
  "v_exp_f32 v34, v34\n" \
  "v_exp_f32 v35, v35\n" \
  "v_exp_f32 v36, v36\n" \
  "v_exp_f32 v37, v37\n" \
  "s_waitcnt lgkmcnt(0)\n" \
  "v_mfma_f32_32x32x16_bf16 v[16:31], v[48:51], v[68:71], v[76:91]\n" \
  "ds_read_b128 v[48:51], v73 offset:41984\n" \
  "ds_read_b128 v[52:55], v75 offset:8448\n" \
  "ds_read_b128 v[56:59], v75 offset:8976\n" \
  "v_exp_f32 v38, v38\n" \
  "v_exp_f32 v39, v39\n" \
  "v_exp_f32 v40, v40\n" \
  "v_exp_f32 v41, v41\n" \
  "v_smfmac_f32_32x32x32_bf16 v[0:15], v[60:63], v[32:39], v72\n" \
  "v_exp_f32 v42, v42\n" \
  "v_exp_f32 v43, v43\n" \
  "v_exp_f32 v44, v44\n" \
  "v_exp_f32 v45, v45\n" \
  "v_exp_f32 v46, v46\n" \
  "v_exp_f32 v47, v47\n" \
  "s_setprio 2\n" \
  "v_exp_f32 v16, v16\n" \
  "v_exp_f32 v17, v17\n" \
  "v_smfmac_f32_32x32x32_bf16 v[0:15], v[64:67], v[40:47], v72\n" \
  "v_exp_f32 v18, v18\n" \
  "v_exp_f32 v19, v19\n" \
  "v_exp_f32 v20, v20\n" \
  "v_exp_f32 v21, v21\n" \
  "s_waitcnt lgkmcnt(0)\n" \
  "v_mfma_f32_32x32x16_bf16 v[32:47], v[48:51], v[68:71], v[76:91]\n" \
  "ds_read_b128 v[48:51], v73 offset:43008\n" \
  "ds_read_b128 v[60:63], v75 offset:9504\n" \
  "ds_read_b128 v[64:67], v75 offset:10032\n" \
  "s_waitcnt vmcnt(5)\n" \
  "ds_write_b128 v95, v[100:103] offset:49152\n" \
  "ds_write_b128 v96, v[104:107] offset:50688\n" \
  "s_add_u32 %[sq], %[sq], 1\n" \
  "s_and_b32 %[sq], %[sq], 7\n" \
  "s_lshl_b32 %[st], %[sq], 13\n" \
  "s_add_u32 %[st], %[st], %[skb]\n" \
  "buffer_load_dwordx4 v[108:111], v94, %[ws], %[st] offen sc1\n" \
  "s_add_u32 %[st], %[st], 0x200000\n" \
  "buffer_load_dwordx4 v[112:115], v94, %[ws], %[st] offen sc1\n" \
  "v_exp_f32 v22, v22\n" \
  "v_exp_f32 v23, v23\n" \
  "v_exp_f32 v24, v24\n" \
  "v_exp_f32 v25, v25\n" \
  "v_smfmac_f32_32x32x32_bf16 v[0:15], v[52:55], v[16:23], v72\n" \
  "v_exp_f32 v26, v26\n" \
  "v_exp_f32 v27, v27\n" \
  "v_exp_f32 v28, v28\n" \
  "v_exp_f32 v29, v29\n" \
  "v_exp_f32 v30, v30\n" \
  "v_exp_f32 v31, v31\n" \
  "v_exp_f32 v32, v32\n" \
  "v_exp_f32 v33, v33\n" \
  "v_smfmac_f32_32x32x32_bf16 v[0:15], v[56:59], v[24:31], v72\n" \
  "v_exp_f32 v34, v34\n" \
  "v_exp_f32 v35, v35\n" \
  "v_exp_f32 v36, v36\n" \
  "v_exp_f32 v37, v37\n" \
  "s_waitcnt lgkmcnt(0)\n" \
  "v_mfma_f32_32x32x16_bf16 v[16:31], v[48:51], v[68:71], v[76:91]\n" \
  "ds_read_b128 v[48:51], v73 offset:44032\n" \
  "ds_read_b128 v[52:55], v75 offset:10560\n" \
  "ds_read_b128 v[56:59], v75 offset:11088\n" \
  "buffer_load_dwordx4 v[200:203], v98, %[refb], %[sef] offen\n" \
  "s_add_u32 %[sef], %[sef], %[sefs]\n" \
  "v_exp_f32 v38, v38\n" \
  "v_exp_f32 v39, v39\n" \
  "v_exp_f32 v40, v40\n" \
  "v_exp_f32 v41, v41\n" \
  "v_smfmac_f32_32x32x32_bf16 v[0:15], v[60:63], v[32:39], v72\n" \
  "v_exp_f32 v42, v42\n" \
  "v_exp_f32 v43, v43\n" \
  "v_exp_f32 v44, v44\n" \
  "v_exp_f32 v45, v45\n" \
  "v_exp_f32 v46, v46\n" \
  "v_exp_f32 v47, v47\n" \
  "s_setprio 1\n" \
  "v_exp_f32 v16, v16\n" \
  "v_exp_f32 v17, v17\n" \
  "v_smfmac_f32_32x32x32_bf16 v[0:15], v[64:67], v[40:47], v72\n" \
  "v_exp_f32 v18, v18\n" \
  "v_exp_f32 v19, v19\n" \
  "v_exp_f32 v20, v20\n" \
  "v_exp_f32 v21, v21\n" \
  "s_waitcnt lgkmcnt(0)\n" \
  "v_mfma_f32_32x32x16_bf16 v[32:47], v[48:51], v[68:71], v[76:91]\n" \
  "ds_read_b128 v[48:51], v73 offset:45056\n" \
  "ds_read_b128 v[60:63], v75 offset:11616\n" \
  "ds_read_b128 v[64:67], v75 offset:12144\n" \
  "buffer_load_dwordx4 v[204:207], v98, %[refb], %[sef] offen\n" \
  "s_add_u32 %[sef], %[sef], %[sefs]\n" \
  "s_add_u32 %[sef], %[sef], %[sefr]\n" \
  "v_exp_f32 v22, v22\n" \
  "v_exp_f32 v23, v23\n" \
  "v_exp_f32 v24, v24\n" \
  "v_exp_f32 v25, v25\n" \
  "v_smfmac_f32_32x32x32_bf16 v[0:15], v[52:55], v[16:23], v72\n" \
  "v_exp_f32 v26, v26\n" \
  "v_exp_f32 v27, v27\n" \
  "v_exp_f32 v28, v28\n" \
  "v_exp_f32 v29, v29\n" \
  "v_exp_f32 v30, v30\n" \
  "v_exp_f32 v31, v31\n" \
  "v_exp_f32 v32, v32\n" \
  "v_exp_f32 v33, v33\n" \
  "v_smfmac_f32_32x32x32_bf16 v[0:15], v[56:59], v[24:31], v72\n" \
  "v_exp_f32 v34, v34\n" \
  "v_exp_f32 v35, v35\n" \
  "v_exp_f32 v36, v36\n" \
  "v_exp_f32 v37, v37\n" \
  "s_waitcnt lgkmcnt(0)\n" \
  "v_mfma_f32_32x32x16_bf16 v[16:31], v[48:51], v[68:71], v[76:91]\n" \
  "ds_read_b128 v[48:51], v73 offset:46080\n" \
  "ds_read_b128 v[52:55], v75 offset:12672\n" \
  "ds_read_b128 v[56:59], v75 offset:13200\n" \
  "buffer_load_dwordx4 v[208:211], v98, %[refc], %[sef] offen\n" \
  "s_add_u32 %[sef], %[sef], %[sefs]\n" \
  "v_exp_f32 v38, v38\n" \
  "v_exp_f32 v39, v39\n" \
  "v_exp_f32 v40, v40\n" \
  "v_exp_f32 v41, v41\n" \
  "v_smfmac_f32_32x32x32_bf16 v[0:15], v[60:63], v[32:39], v72\n" \
  "v_exp_f32 v42, v42\n" \
  "v_exp_f32 v43, v43\n" \
  "v_exp_f32 v44, v44\n" \
  "v_exp_f32 v45, v45\n" \
  "v_exp_f32 v46, v46\n" \
  "v_exp_f32 v47, v47\n" \
  "s_setprio 0\n" \
  "v_exp_f32 v16, v16\n" \
  "v_exp_f32 v17, v17\n" \
  "v_smfmac_f32_32x32x32_bf16 v[0:15], v[64:67], v[40:47], v72\n" \
  "v_exp_f32 v18, v18\n" \
  "v_exp_f32 v19, v19\n" \
  "v_exp_f32 v20, v20\n" \
  "v_exp_f32 v21, v21\n" \
  "s_waitcnt lgkmcnt(0)\n" \
  "v_mfma_f32_32x32x16_bf16 v[32:47], v[48:51], v[68:71], v[76:91]\n" \
  "ds_read_b128 v[48:51], v73 offset:47104\n" \
  "ds_read_b128 v[60:63], v75 offset:13728\n" \
  "ds_read_b128 v[64:67], v75 offset:14256\n" \
  "buffer_load_dwordx4 v[212:215], v98, %[refc], %[sef] offen\n" \
  "s_add_u32 %[sef], %[sef], %[sefs]\n" \
  "v_exp_f32 v22, v22\n" \
  "v_exp_f32 v23, v23\n" \
  "v_exp_f32 v24, v24\n" \
  "v_exp_f32 v25, v25\n" \
  "v_smfmac_f32_32x32x32_bf16 v[0:15], v[52:55], v[16:23], v72\n" \
  "v_exp_f32 v26, v26\n" \
  "v_exp_f32 v27, v27\n" \
  "v_exp_f32 v28, v28\n" \
  "v_exp_f32 v29, v29\n" \
  "v_exp_f32 v30, v30\n" \
  "v_exp_f32 v31, v31\n" \
  "v_exp_f32 v32, v32\n" \
  "v_exp_f32 v33, v33\n" \
  "v_smfmac_f32_32x32x32_bf16 v[0:15], v[56:59], v[24:31], v72\n" \
  "v_exp_f32 v34, v34\n" \
  "v_exp_f32 v35, v35\n" \
  "v_exp_f32 v36, v36\n" \
  "v_exp_f32 v37, v37\n" \
  "s_waitcnt lgkmcnt(0)\n" \
  "v_mfma_f32_32x32x16_bf16 v[16:31], v[48:51], v[68:71], v[76:91]\n" \
  "ds_read_b128 v[48:51], v73 offset:48128\n" \
  "ds_read_b128 v[52:55], v75 offset:14784\n" \
  "ds_read_b128 v[56:59], v75 offset:15312\n" \
  "s_barrier\n" \
  "v_exp_f32 v38, v38\n" \
  "v_exp_f32 v39, v39\n" \
  "v_exp_f32 v40, v40\n" \
  "v_exp_f32 v41, v41\n" \
  "v_smfmac_f32_32x32x32_bf16 v[0:15], v[60:63], v[32:39], v72\n" \
  "v_exp_f32 v42, v42\n" \
  "v_exp_f32 v43, v43\n" \
  "v_exp_f32 v44, v44\n" \
  "v_exp_f32 v45, v45\n" \
  "v_exp_f32 v46, v46\n" \
  "v_exp_f32 v47, v47\n" \
  "s_setprio 3\n" \
  "v_exp_f32 v16, v16\n" \
  "v_exp_f32 v17, v17\n" \
  "v_smfmac_f32_32x32x32_bf16 v[0:15], v[64:67], v[40:47], v72\n" \
  "v_exp_f32 v18, v18\n" \
  "v_exp_f32 v19, v19\n" \
  "v_exp_f32 v20, v20\n" \
  "v_exp_f32 v21, v21\n" \
  "s_waitcnt lgkmcnt(0)\n" \
  "v_mfma_f32_32x32x16_bf16 v[32:47], v[48:51], v[68:71], v[76:91]\n" \
  "ds_read_b128 v[48:51], v73 offset:49152\n" \
  "ds_read_b128 v[60:63], v75 offset:15840\n" \
  "ds_read_b128 v[64:67], v75 offset:16368\n" \
  "buffer_load_dwordx4 v[216:219], v98, %[refc], %[sef] offen\n" \
  "s_add_u32 %[sef], %[sef], %[sefs]\n" \
  "v_exp_f32 v22, v22\n" \
  "v_exp_f32 v23, v23\n" \
  "v_exp_f32 v24, v24\n" \
  "v_exp_f32 v25, v25\n" \
  "v_smfmac_f32_32x32x32_bf16 v[0:15], v[52:55], v[16:23], v72\n" \
  "v_exp_f32 v26, v26\n" \
  "v_exp_f32 v27, v27\n" \
  "v_exp_f32 v28, v28\n" \
  "v_exp_f32 v29, v29\n" \
  "v_exp_f32 v30, v30\n" \
  "v_exp_f32 v31, v31\n" \
  "v_exp_f32 v32, v32\n" \
  "v_exp_f32 v33, v33\n" \
  "v_smfmac_f32_32x32x32_bf16 v[0:15], v[56:59], v[24:31], v72\n" \
  "v_exp_f32 v34, v34\n" \
  "v_exp_f32 v35, v35\n" \
  "v_exp_f32 v36, v36\n" \
  "v_exp_f32 v37, v37\n" \
  "s_waitcnt lgkmcnt(0)\n" \
  "v_mfma_f32_32x32x16_bf16 v[16:31], v[48:51], v[68:71], v[76:91]\n" \
  "ds_read_b128 v[48:51], v73 offset:50176\n" \
  "ds_read_b128 v[52:55], v75 offset:16896\n" \
  "ds_read_b128 v[56:59], v75 offset:17424\n" \
  "v_exp_f32 v38, v38\n" \
  "v_exp_f32 v39, v39\n" \
  "v_exp_f32 v40, v40\n" \
  "v_exp_f32 v41, v41\n" \
  "v_smfmac_f32_32x32x32_bf16 v[0:15], v[60:63], v[32:39], v72\n" \
  "v_exp_f32 v42, v42\n" \
  "v_exp_f32 v43, v43\n" \
  "v_exp_f32 v44, v44\n" \
  "v_exp_f32 v45, v45\n" \
  "v_exp_f32 v46, v46\n" \
  "v_exp_f32 v47, v47\n" \
  "s_setprio 2\n" \
  "v_exp_f32 v16, v16\n" \
  "v_exp_f32 v17, v17\n" \
  "v_smfmac_f32_32x32x32_bf16 v[0:15], v[64:67], v[40:47], v72\n" \
  "v_exp_f32 v18, v18\n" \
  "v_exp_f32 v19, v19\n" \
  "v_exp_f32 v20, v20\n" \
  "v_exp_f32 v21, v21\n" \
  "s_waitcnt lgkmcnt(0)\n" \
  "v_mfma_f32_32x32x16_bf16 v[32:47], v[48:51], v[68:71], v[76:91]\n" \
  "ds_read_b128 v[48:51], v73 offset:51200\n" \
  "ds_read_b128 v[60:63], v75 offset:17952\n" \
  "ds_read_b128 v[64:67], v75 offset:18480\n" \
  "s_waitcnt vmcnt(5)\n" \
  "ds_write_b128 v95, v[108:111] offset:57344\n" \
  "ds_write_b128 v96, v[112:115] offset:59136\n" \
  "v_exp_f32 v22, v22\n" \
  "v_exp_f32 v23, v23\n" \
  "v_exp_f32 v24, v24\n" \
  "v_exp_f32 v25, v25\n" \
  "v_smfmac_f32_32x32x32_bf16 v[0:15], v[52:55], v[16:23], v72\n" \
  "v_exp_f32 v26, v26\n" \
  "v_exp_f32 v27, v27\n" \
  "v_exp_f32 v28, v28\n" \
  "v_exp_f32 v29, v29\n" \
  "v_exp_f32 v30, v30\n" \
  "v_exp_f32 v31, v31\n" \
  "v_exp_f32 v32, v32\n" \
  "v_exp_f32 v33, v33\n" \
  "v_smfmac_f32_32x32x32_bf16 v[0:15], v[56:59], v[24:31], v72\n" \
  "v_exp_f32 v34, v34\n" \
  "v_exp_f32 v35, v35\n" \
  "v_exp_f32 v36, v36\n" \
  "v_exp_f32 v37, v37\n" \
  "s_waitcnt lgkmcnt(0)\n" \
  "v_mfma_f32_32x32x16_bf16 v[16:31], v[48:51], v[68:71], v[76:91]\n" \
  "ds_read_b128 v[48:51], v73 offset:52224\n" \
  "ds_read_b128 v[52:55], v75 offset:19008\n" \
  "ds_read_b128 v[56:59], v75 offset:19536\n" \
  "v_exp_f32 v38, v38\n" \
  "v_exp_f32 v39, v39\n" \
  "v_exp_f32 v40, v40\n" \
  "v_exp_f32 v41, v41\n" \
  "v_smfmac_f32_32x32x32_bf16 v[0:15], v[60:63], v[32:39], v72\n" \
  "v_exp_f32 v42, v42\n" \
  "v_exp_f32 v43, v43\n" \
  "v_exp_f32 v44, v44\n" \
  "v_exp_f32 v45, v45\n" \
  "v_exp_f32 v46, v46\n" \
  "v_exp_f32 v47, v47\n" \
  "s_setprio 1\n" \
  "v_exp_f32 v16, v16\n" \
  "v_exp_f32 v17, v17\n" \
  "v_smfmac_f32_32x32x32_bf16 v[0:15], v[64:67], v[40:47], v72\n" \
  "v_exp_f32 v18, v18\n" \
  "v_exp_f32 v19, v19\n" \
  "v_exp_f32 v20, v20\n" \
  "v_exp_f32 v21, v21\n" \
  "s_waitcnt lgkmcnt(0)\n" \
  "v_mfma_f32_32x32x16_bf16 v[32:47], v[48:51], v[68:71], v[76:91]\n" \
  "ds_read_b128 v[48:51], v73 offset:53248\n" \
  "ds_read_b128 v[60:63], v75 offset:20064\n" \
  "ds_read_b128 v[64:67], v75 offset:20592\n" \
  "v_exp_f32 v22, v22\n" \
  "v_exp_f32 v23, v23\n" \
  "v_exp_f32 v24, v24\n" \
  "v_exp_f32 v25, v25\n" \
  "v_smfmac_f32_32x32x32_bf16 v[0:15], v[52:55], v[16:23], v72\n" \
  "v_exp_f32 v26, v26\n" \
  "v_exp_f32 v27, v27\n" \
  "v_exp_f32 v28, v28\n" \
  "v_exp_f32 v29, v29\n" \
  "v_exp_f32 v30, v30\n" \
  "v_exp_f32 v31, v31\n" \
  "v_exp_f32 v32, v32\n" \
  "v_exp_f32 v33, v33\n" \
  "v_smfmac_f32_32x32x32_bf16 v[0:15], v[56:59], v[24:31], v72\n" \
  "v_exp_f32 v34, v34\n" \
  "v_exp_f32 v35, v35\n" \
  "v_exp_f32 v36, v36\n" \
  "v_exp_f32 v37, v37\n" \
  "s_waitcnt lgkmcnt(0)\n" \
  "v_mfma_f32_32x32x16_bf16 v[16:31], v[48:51], v[68:71], v[76:91]\n" \
  "ds_read_b128 v[48:51], v73 offset:54272\n" \
  "ds_read_b128 v[52:55], v75 offset:21120\n" \
  "ds_read_b128 v[56:59], v75 offset:21648\n" \
  "v_exp_f32 v38, v38\n" \
  "v_exp_f32 v39, v39\n" \
  "v_exp_f32 v40, v40\n" \
  "v_exp_f32 v41, v41\n" \
  "v_smfmac_f32_32x32x32_bf16 v[0:15], v[60:63], v[32:39], v72\n" \
  "v_exp_f32 v42, v42\n" \
  "v_exp_f32 v43, v43\n" \
  "v_exp_f32 v44, v44\n" \
  "v_exp_f32 v45, v45\n" \
  "v_exp_f32 v46, v46\n" \
  "v_exp_f32 v47, v47\n" \
  "s_setprio 0\n" \
  "v_exp_f32 v16, v16\n" \
  "v_exp_f32 v17, v17\n" \
  "v_smfmac_f32_32x32x32_bf16 v[0:15], v[64:67], v[40:47], v72\n" \
  "v_exp_f32 v18, v18\n" \
  "v_exp_f32 v19, v19\n" \
  "v_exp_f32 v20, v20\n" \
  "v_exp_f32 v21, v21\n" \
  "s_waitcnt lgkmcnt(0)\n" \
  "v_mfma_f32_32x32x16_bf16 v[32:47], v[48:51], v[68:71], v[76:91]\n" \
  "ds_read_b128 v[48:51], v73 offset:55296\n" \
  "ds_read_b128 v[60:63], v75 offset:22176\n" \
  "ds_read_b128 v[64:67], v75 offset:22704\n" \
  "v_exp_f32 v22, v22\n" \
  "v_exp_f32 v23, v23\n" \
  "v_exp_f32 v24, v24\n" \
  "v_exp_f32 v25, v25\n" \
  "v_smfmac_f32_32x32x32_bf16 v[0:15], v[52:55], v[16:23], v72\n" \
  "v_exp_f32 v26, v26\n" \
  "v_exp_f32 v27, v27\n" \
  "v_exp_f32 v28, v28\n" \
  "v_exp_f32 v29, v29\n" \
  "v_exp_f32 v30, v30\n" \
  "v_exp_f32 v31, v31\n" \
  "v_exp_f32 v32, v32\n" \
  "v_exp_f32 v33, v33\n" \
  "v_smfmac_f32_32x32x32_bf16 v[0:15], v[56:59], v[24:31], v72\n" \
  "v_exp_f32 v34, v34\n" \
  "v_exp_f32 v35, v35\n" \
  "v_exp_f32 v36, v36\n" \
  "v_exp_f32 v37, v37\n" \
  "s_waitcnt lgkmcnt(0)\n" \
  "v_mfma_f32_32x32x16_bf16 v[16:31], v[48:51], v[68:71], v[76:91]\n" \
  "ds_read_b128 v[48:51], v73 offset:56320\n" \
  "ds_read_b128 v[52:55], v75 offset:23232\n" \
  "ds_read_b128 v[56:59], v75 offset:23760\n" \
  "s_barrier\n" \
  "v_exp_f32 v38, v38\n" \
  "v_exp_f32 v39, v39\n" \
  "v_exp_f32 v40, v40\n" \
  "v_exp_f32 v41, v41\n" \
  "v_smfmac_f32_32x32x32_bf16 v[0:15], v[60:63], v[32:39], v72\n" \
  "v_exp_f32 v42, v42\n" \
  "v_exp_f32 v43, v43\n" \
  "v_exp_f32 v44, v44\n" \
  "v_exp_f32 v45, v45\n" \
  "v_exp_f32 v46, v46\n" \
  "v_exp_f32 v47, v47\n" \
  "s_setprio 3\n" \
  "v_exp_f32 v16, v16\n" \
  "v_exp_f32 v17, v17\n" \
  "v_smfmac_f32_32x32x32_bf16 v[0:15], v[64:67], v[40:47], v72\n" \
  "v_exp_f32 v18, v18\n" \
  "v_exp_f32 v19, v19\n" \
  "v_exp_f32 v20, v20\n" \
  "v_exp_f32 v21, v21\n" \
  "s_waitcnt lgkmcnt(0)\n" \
  "v_mfma_f32_32x32x16_bf16 v[32:47], v[48:51], v[68:71], v[76:91]\n" \
  "ds_read_b128 v[48:51], v73 offset:57344\n" \
  "ds_read_b128 v[60:63], v75 offset:24288\n" \
  "ds_read_b128 v[64:67], v75 offset:24816\n" \
  "v_exp_f32 v22, v22\n" \
  "v_exp_f32 v23, v23\n" \
  "v_exp_f32 v24, v24\n" \
  "v_exp_f32 v25, v25\n" \
  "v_smfmac_f32_32x32x32_bf16 v[0:15], v[52:55], v[16:23], v72\n" \
  "v_exp_f32 v26, v26\n" \
  "v_exp_f32 v27, v27\n" \
  "v_exp_f32 v28, v28\n" \
  "v_exp_f32 v29, v29\n" \
  "v_exp_f32 v30, v30\n" \
  "v_exp_f32 v31, v31\n" \
  "v_exp_f32 v32, v32\n" \
  "v_exp_f32 v33, v33\n" \
  "v_smfmac_f32_32x32x32_bf16 v[0:15], v[56:59], v[24:31], v72\n" \
  "v_exp_f32 v34, v34\n" \
  "v_exp_f32 v35, v35\n" \
  "v_exp_f32 v36, v36\n" \
  "v_exp_f32 v37, v37\n" \
  "s_waitcnt lgkmcnt(0)\n" \
  "v_mfma_f32_32x32x16_bf16 v[16:31], v[48:51], v[68:71], v[76:91]\n" \
  "ds_read_b128 v[48:51], v73 offset:58368\n" \
  "ds_read_b128 v[52:55], v75 offset:25344\n" \
  "ds_read_b128 v[56:59], v75 offset:25872\n" \
  "v_exp_f32 v38, v38\n" \
  "v_exp_f32 v39, v39\n" \
  "v_exp_f32 v40, v40\n" \
  "v_exp_f32 v41, v41\n" \
  "v_smfmac_f32_32x32x32_bf16 v[0:15], v[60:63], v[32:39], v72\n" \
  "v_exp_f32 v42, v42\n" \
  "v_exp_f32 v43, v43\n" \
  "v_exp_f32 v44, v44\n" \
  "v_exp_f32 v45, v45\n" \
  "v_exp_f32 v46, v46\n" \
  "v_exp_f32 v47, v47\n" \
  "s_setprio 2\n" \
  "v_exp_f32 v16, v16\n" \
  "v_exp_f32 v17, v17\n" \
  "v_smfmac_f32_32x32x32_bf16 v[0:15], v[64:67], v[40:47], v72\n" \
  "v_exp_f32 v18, v18\n" \
  "v_exp_f32 v19, v19\n" \
  "v_exp_f32 v20, v20\n" \
  "v_exp_f32 v21, v21\n" \
  "s_waitcnt lgkmcnt(0)\n" \
  "v_mfma_f32_32x32x16_bf16 v[32:47], v[48:51], v[68:71], v[76:91]\n" \
  "ds_read_b128 v[48:51], v73 offset:59392\n" \
  "ds_read_b128 v[60:63], v75 offset:26400\n" \
  "ds_read_b128 v[64:67], v75 offset:26928\n" \
  "v_exp_f32 v22, v22\n" \
  "v_exp_f32 v23, v23\n" \
  "v_exp_f32 v24, v24\n" \
  "v_exp_f32 v25, v25\n" \
  "v_smfmac_f32_32x32x32_bf16 v[0:15], v[52:55], v[16:23], v72\n" \
  "v_exp_f32 v26, v26\n" \
  "v_exp_f32 v27, v27\n" \
  "v_exp_f32 v28, v28\n" \
  "v_exp_f32 v29, v29\n" \
  "v_exp_f32 v30, v30\n" \
  "v_exp_f32 v31, v31\n" \
  "v_exp_f32 v32, v32\n" \
  "v_exp_f32 v33, v33\n" \
  "v_smfmac_f32_32x32x32_bf16 v[0:15], v[56:59], v[24:31], v72\n" \
  "v_exp_f32 v34, v34\n" \
  "v_exp_f32 v35, v35\n" \
  "v_exp_f32 v36, v36\n" \
  "v_exp_f32 v37, v37\n" \
  "s_waitcnt lgkmcnt(0)\n" \
  "v_mfma_f32_32x32x16_bf16 v[16:31], v[48:51], v[68:71], v[76:91]\n" \
  "ds_read_b128 v[48:51], v73 offset:60416\n" \
  "ds_read_b128 v[52:55], v75 offset:27456\n" \
  "ds_read_b128 v[56:59], v75 offset:27984\n" \
  "v_exp_f32 v38, v38\n" \
  "v_exp_f32 v39, v39\n" \
  "v_exp_f32 v40, v40\n" \
  "v_exp_f32 v41, v41\n" \
  "v_smfmac_f32_32x32x32_bf16 v[0:15], v[60:63], v[32:39], v72\n" \
  "v_exp_f32 v42, v42\n" \
  "v_exp_f32 v43, v43\n" \
  "v_exp_f32 v44, v44\n" \
  "v_exp_f32 v45, v45\n" \
  "v_exp_f32 v46, v46\n" \
  "v_exp_f32 v47, v47\n" \
  "s_setprio 1\n" \
  "v_exp_f32 v16, v16\n" \
  "v_exp_f32 v17, v17\n" \
  "v_smfmac_f32_32x32x32_bf16 v[0:15], v[64:67], v[40:47], v72\n" \
  "v_exp_f32 v18, v18\n" \
  "v_exp_f32 v19, v19\n" \
  "v_exp_f32 v20, v20\n" \
  "v_exp_f32 v21, v21\n" \
  "s_waitcnt lgkmcnt(0)\n" \
  "v_mfma_f32_32x32x16_bf16 v[32:47], v[48:51], v[68:71], v[76:91]\n" \
  "ds_read_b128 v[48:51], v73 offset:61440\n" \
  "ds_read_b128 v[60:63], v75 offset:28512\n" \
  "ds_read_b128 v[64:67], v75 offset:29040\n" \
  "v_exp_f32 v22, v22\n" \
  "v_exp_f32 v23, v23\n" \
  "v_exp_f32 v24, v24\n" \
  "v_exp_f32 v25, v25\n" \
  "v_smfmac_f32_32x32x32_bf16 v[0:15], v[52:55], v[16:23], v72\n" \
  "v_exp_f32 v26, v26\n" \
  "v_exp_f32 v27, v27\n" \
  "v_exp_f32 v28, v28\n" \
  "v_exp_f32 v29, v29\n" \
  "v_exp_f32 v30, v30\n" \
  "v_exp_f32 v31, v31\n" \
  "v_exp_f32 v32, v32\n" \
  "v_exp_f32 v33, v33\n" \
  "v_smfmac_f32_32x32x32_bf16 v[0:15], v[56:59], v[24:31], v72\n" \
  "v_exp_f32 v34, v34\n" \
  "v_exp_f32 v35, v35\n" \
  "v_exp_f32 v36, v36\n" \
  "v_exp_f32 v37, v37\n" \
  "s_waitcnt lgkmcnt(0)\n" \
  "v_mfma_f32_32x32x16_bf16 v[16:31], v[48:51], v[68:71], v[76:91]\n" \
  "ds_read_b128 v[48:51], v73 offset:62464\n" \
  "ds_read_b128 v[52:55], v75 offset:29568\n" \
  "ds_read_b128 v[56:59], v75 offset:30096\n" \
  "v_exp_f32 v38, v38\n" \
  "v_exp_f32 v39, v39\n" \
  "v_exp_f32 v40, v40\n" \
  "v_exp_f32 v41, v41\n" \
  "v_smfmac_f32_32x32x32_bf16 v[0:15], v[60:63], v[32:39], v72\n" \
  "v_exp_f32 v42, v42\n" \
  "v_exp_f32 v43, v43\n" \
  "v_exp_f32 v44, v44\n" \
  "v_exp_f32 v45, v45\n" \
  "v_exp_f32 v46, v46\n" \
  "v_exp_f32 v47, v47\n" \
  "s_setprio 0\n" \
  "v_exp_f32 v16, v16\n" \
  "v_exp_f32 v17, v17\n" \
  "v_smfmac_f32_32x32x32_bf16 v[0:15], v[64:67], v[40:47], v72\n" \
  "v_exp_f32 v18, v18\n" \
  "v_exp_f32 v19, v19\n" \
  "v_exp_f32 v20, v20\n" \
  "v_exp_f32 v21, v21\n" \
  "s_waitcnt lgkmcnt(0)\n" \
  "v_mfma_f32_32x32x16_bf16 v[32:47], v[48:51], v[68:71], v[76:91]\n" \
  "ds_read_b128 v[48:51], v73 offset:63488\n" \
  "ds_read_b128 v[60:63], v75 offset:30624\n" \
  "ds_read_b128 v[64:67], v75 offset:31152\n" \
  "v_exp_f32 v22, v22\n" \
  "v_exp_f32 v23, v23\n" \
  "v_exp_f32 v24, v24\n" \
  "v_exp_f32 v25, v25\n" \
  "v_smfmac_f32_32x32x32_bf16 v[0:15], v[52:55], v[16:23], v72\n" \
  "v_exp_f32 v26, v26\n" \
  "v_exp_f32 v27, v27\n" \
  "v_exp_f32 v28, v28\n" \
  "v_exp_f32 v29, v29\n" \
  "v_exp_f32 v30, v30\n" \
  "v_exp_f32 v31, v31\n" \
  "v_exp_f32 v32, v32\n" \
  "v_exp_f32 v33, v33\n" \
  "v_smfmac_f32_32x32x32_bf16 v[0:15], v[56:59], v[24:31], v72\n" \
  "v_exp_f32 v34, v34\n" \
  "v_exp_f32 v35, v35\n" \
  "v_exp_f32 v36, v36\n" \
  "v_exp_f32 v37, v37\n" \
  "s_waitcnt lgkmcnt(0)\n" \
  "v_mfma_f32_32x32x16_bf16 v[16:31], v[48:51], v[68:71], v[76:91]\n" \
  "ds_read_b128 v[48:51], v73 offset:64512\n" \
  "ds_read_b128 v[52:55], v75 offset:31680\n" \
  "ds_read_b128 v[56:59], v75 offset:32208\n" \
  "v_exp_f32 v38, v38\n" \
  "v_exp_f32 v39, v39\n" \
  "v_exp_f32 v40, v40\n" \
  "v_exp_f32 v41, v41\n" \
  "v_smfmac_f32_32x32x32_bf16 v[0:15], v[60:63], v[32:39], v72\n" \
  "v_exp_f32 v42, v42\n" \
  "v_exp_f32 v43, v43\n" \
  "v_exp_f32 v44, v44\n" \
  "v_exp_f32 v45, v45\n" \
  "v_exp_f32 v46, v46\n" \
  "v_exp_f32 v47, v47\n" \
  "s_setprio 3\n" \
  "v_exp_f32 v16, v16\n" \
  "v_exp_f32 v17, v17\n" \
  "v_smfmac_f32_32x32x32_bf16 v[0:15], v[64:67], v[40:47], v72\n" \
  "v_exp_f32 v18, v18\n" \
  "v_exp_f32 v19, v19\n" \
  "v_exp_f32 v20, v20\n" \
  "v_exp_f32 v21, v21\n" \
  "s_waitcnt lgkmcnt(0)\n" \
  "v_mfma_f32_32x32x16_bf16 v[32:47], v[48:51], v[68:71], v[76:91]\n" \
  "ds_read_b128 v[60:63], v75 offset:32736\n" \
  "ds_read_b128 v[64:67], v75 offset:33264\n" \
  "v_exp_f32 v22, v22\n" \
  "v_exp_f32 v23, v23\n" \
  "v_exp_f32 v24, v24\n" \
  "v_exp_f32 v25, v25\n" \
  "v_smfmac_f32_32x32x32_bf16 v[0:15], v[52:55], v[16:23], v72\n" \
  "v_exp_f32 v26, v26\n" \
  "v_exp_f32 v27, v27\n" \
  "v_exp_f32 v28, v28\n" \
  "v_exp_f32 v29, v29\n" \
  "v_exp_f32 v30, v30\n" \
  "v_exp_f32 v31, v31\n" \
  "v_exp_f32 v32, v32\n" \
  "v_exp_f32 v33, v33\n" \
  "v_smfmac_f32_32x32x32_bf16 v[0:15], v[56:59], v[24:31], v72\n" \
  "v_exp_f32 v34, v34\n" \
  "v_exp_f32 v35, v35\n" \
  "v_exp_f32 v36, v36\n" \
  "v_exp_f32 v37, v37\n" \
  "s_waitcnt lgkmcnt(0)\n" \
  "v_exp_f32 v38, v38\n" \
  "v_exp_f32 v39, v39\n" \
  "v_exp_f32 v40, v40\n" \
  "v_exp_f32 v41, v41\n" \
  "v_smfmac_f32_32x32x32_bf16 v[0:15], v[60:63], v[32:39], v72\n" \
  "v_exp_f32 v42, v42\n" \
  "v_exp_f32 v43, v43\n" \
  "v_exp_f32 v44, v44\n" \
  "v_exp_f32 v45, v45\n" \
  "v_exp_f32 v46, v46\n" \
  "v_exp_f32 v47, v47\n" \
  "s_nop 1\n" \
  "v_smfmac_f32_32x32x32_bf16 v[0:15], v[64:67], v[40:47], v72\n" \
  "s_nop 7\n" \
  "s_nop 7\n" \
  "s_setprio 0\n" \
  "s_waitcnt vmcnt(0)\n"
#define ATTN_F \
  "ds_read_b128 v[48:51], v73 offset:0\n" \
  "ds_read_b128 v[52:55], v74 offset:0\n" \
  "ds_read_b128 v[56:59], v74 offset:528\n" \
  "s_waitcnt lgkmcnt(2)\n" \
  "v_mfma_f32_32x32x16_bf16 v[16:31], v[48:51], v[68:71], v[76:91]\n" \
  "ds_read_b128 v[48:51], v73 offset:1024\n" \
  "v_mov_b32 v40, 0\n" \
  "v_mov_b32 v41, 0\n" \
  "v_mov_b32 v42, 0\n" \
  "v_mov_b32 v43, 0\n" \
  "v_mov_b32 v44, 0\n" \
  "v_mov_b32 v45, 0\n" \
  "v_mov_b32 v46, 0\n" \
  "v_mov_b32 v47, 0\n" \
  "v_mov_b32 v64, 0\n" \
  "v_mov_b32 v65, 0\n" \
  "v_mov_b32 v66, 0\n" \
  "v_mov_b32 v67, 0\n" \
  "s_nop 1\n" \
  "1:\n" \
  "v_exp_f32 v16, v16\n" \
  "v_exp_f32 v17, v17\n" \
  "v_smfmac_f32_32x32x32_bf16 v[0:15], v[64:67], v[40:47], v72\n" \
  "v_exp_f32 v18, v18\n" \
  "v_exp_f32 v19, v19\n" \
  "v_exp_f32 v20, v20\n" \
  "v_exp_f32 v21, v21\n" \
  "s_waitcnt lgkmcnt(0)\n" \
  "v_mfma_f32_32x32x16_bf16 v[32:47], v[48:51], v[68:71], v[76:91]\n" \
  "ds_read_b128 v[48:51], v73 offset:2048\n" \
  "ds_read_b128 v[60:63], v74 offset:1056\n" \
  "ds_read_b128 v[64:67], v74 offset:1584\n" \
  "v_exp_f32 v22, v22\n" \
  "v_exp_f32 v23, v23\n" \
  "v_exp_f32 v24, v24\n" \
  "v_exp_f32 v25, v25\n" \
  "v_smfmac_f32_32x32x32_bf16 v[0:15], v[52:55], v[16:23], v72\n" \
  "v_exp_f32 v26, v26\n" \
  "v_exp_f32 v27, v27\n" \
  "v_exp_f32 v28, v28\n" \
  "v_exp_f32 v29, v29\n" \
  "v_exp_f32 v30, v30\n" \
  "v_exp_f32 v31, v31\n" \
  "v_exp_f32 v32, v32\n" \
  "v_exp_f32 v33, v33\n" \
  "v_smfmac_f32_32x32x32_bf16 v[0:15], v[56:59], v[24:31], v72\n" \
  "v_exp_f32 v34, v34\n" \
  "v_exp_f32 v35, v35\n" \
  "v_exp_f32 v36, v36\n" \
  "v_exp_f32 v37, v37\n" \
  "s_waitcnt lgkmcnt(0)\n" \
  "v_mfma_f32_32x32x16_bf16 v[16:31], v[48:51], v[68:71], v[76:91]\n" \
  "ds_read_b128 v[48:51], v73 offset:3072\n" \
  "ds_read_b128 v[52:55], v74 offset:2112\n" \
  "ds_read_b128 v[56:59], v74 offset:2640\n" \
  "v_exp_f32 v38, v38\n" \
  "v_exp_f32 v39, v39\n" \
  "v_exp_f32 v40, v40\n" \
  "v_exp_f32 v41, v41\n" \
  "v_smfmac_f32_32x32x32_bf16 v[0:15], v[60:63], v[32:39], v72\n" \
  "v_exp_f32 v42, v42\n" \
  "v_exp_f32 v43, v43\n" \
  "v_exp_f32 v44, v44\n" \
  "v_exp_f32 v45, v45\n" \
  "v_exp_f32 v46, v46\n" \
  "v_exp_f32 v47, v47\n" \
  "v_exp_f32 v16, v16\n" \
  "v_exp_f32 v17, v17\n" \
  "v_smfmac_f32_32x32x32_bf16 v[0:15], v[64:67], v[40:47], v72\n" \
  "v_exp_f32 v18, v18\n" \
  "v_exp_f32 v19, v19\n" \
  "v_exp_f32 v20, v20\n" \
  "v_exp_f32 v21, v21\n" \
  "s_waitcnt lgkmcnt(0)\n" \
  "v_mfma_f32_32x32x16_bf16 v[32:47], v[48:51], v[68:71], v[76:91]\n" \
  "ds_read_b128 v[48:51], v73 offset:4096\n" \
  "ds_read_b128 v[60:63], v74 offset:3168\n" \
  "ds_read_b128 v[64:67], v74 offset:3696\n" \
  "v_exp_f32 v22, v22\n" \
  "v_exp_f32 v23, v23\n" \
  "v_exp_f32 v24, v24\n" \
  "v_exp_f32 v25, v25\n" \
  "v_smfmac_f32_32x32x32_bf16 v[0:15], v[52:55], v[16:23], v72\n" \
  "v_exp_f32 v26, v26\n" \
  "v_exp_f32 v27, v27\n" \
  "v_exp_f32 v28, v28\n" \
  "v_exp_f32 v29, v29\n" \
  "v_exp_f32 v30, v30\n" \
  "v_exp_f32 v31, v31\n" \
  "v_exp_f32 v32, v32\n" \
  "v_exp_f32 v33, v33\n" \
  "v_smfmac_f32_32x32x32_bf16 v[0:15], v[56:59], v[24:31], v72\n" \
  "v_exp_f32 v34, v34\n" \
  "v_exp_f32 v35, v35\n" \
  "v_exp_f32 v36, v36\n" \
  "v_exp_f32 v37, v37\n" \
  "s_waitcnt lgkmcnt(0)\n" \
  "v_mfma_f32_32x32x16_bf16 v[16:31], v[48:51], v[68:71], v[76:91]\n" \
  "ds_read_b128 v[48:51], v73 offset:5120\n" \
  "ds_read_b128 v[52:55], v74 offset:4224\n" \
  "ds_read_b128 v[56:59], v74 offset:4752\n" \
  "v_exp_f32 v38, v38\n" \
  "v_exp_f32 v39, v39\n" \
  "v_exp_f32 v40, v40\n" \
  "v_exp_f32 v41, v41\n" \
  "v_smfmac_f32_32x32x32_bf16 v[0:15], v[60:63], v[32:39], v72\n" \
  "v_exp_f32 v42, v42\n" \
  "v_exp_f32 v43, v43\n" \
  "v_exp_f32 v44, v44\n" \
  "v_exp_f32 v45, v45\n" \
  "v_exp_f32 v46, v46\n" \
  "v_exp_f32 v47, v47\n" \
  "v_exp_f32 v16, v16\n" \
  "v_exp_f32 v17, v17\n" \
  "v_smfmac_f32_32x32x32_bf16 v[0:15], v[64:67], v[40:47], v72\n" \
  "v_exp_f32 v18, v18\n" \
  "v_exp_f32 v19, v19\n" \
  "v_exp_f32 v20, v20\n" \
  "v_exp_f32 v21, v21\n" \
  "s_waitcnt lgkmcnt(0)\n" \
  "v_mfma_f32_32x32x16_bf16 v[32:47], v[48:51], v[68:71], v[76:91]\n" \
  "ds_read_b128 v[48:51], v73 offset:6144\n" \
  "ds_read_b128 v[60:63], v74 offset:5280\n" \
  "ds_read_b128 v[64:67], v74 offset:5808\n" \
  "v_exp_f32 v22, v22\n" \
  "v_exp_f32 v23, v23\n" \
  "v_exp_f32 v24, v24\n" \
  "v_exp_f32 v25, v25\n" \
  "v_smfmac_f32_32x32x32_bf16 v[0:15], v[52:55], v[16:23], v72\n" \
  "v_exp_f32 v26, v26\n" \
  "v_exp_f32 v27, v27\n" \
  "v_exp_f32 v28, v28\n" \
  "v_exp_f32 v29, v29\n" \
  "v_exp_f32 v30, v30\n" \
  "v_exp_f32 v31, v31\n" \
  "v_exp_f32 v32, v32\n" \
  "v_exp_f32 v33, v33\n" \
  "v_smfmac_f32_32x32x32_bf16 v[0:15], v[56:59], v[24:31], v72\n" \
  "v_exp_f32 v34, v34\n" \
  "v_exp_f32 v35, v35\n" \
  "v_exp_f32 v36, v36\n" \
  "v_exp_f32 v37, v37\n" \
  "s_waitcnt lgkmcnt(0)\n" \
  "v_mfma_f32_32x32x16_bf16 v[16:31], v[48:51], v[68:71], v[76:91]\n" \
  "ds_read_b128 v[48:51], v73 offset:7168\n" \
  "ds_read_b128 v[52:55], v74 offset:6336\n" \
  "ds_read_b128 v[56:59], v74 offset:6864\n" \
  "v_exp_f32 v38, v38\n" \
  "v_exp_f32 v39, v39\n" \
  "v_exp_f32 v40, v40\n" \
  "v_exp_f32 v41, v41\n" \
  "v_smfmac_f32_32x32x32_bf16 v[0:15], v[60:63], v[32:39], v72\n" \
  "v_exp_f32 v42, v42\n" \
  "v_exp_f32 v43, v43\n" \
  "v_exp_f32 v44, v44\n" \
  "v_exp_f32 v45, v45\n" \
  "v_exp_f32 v46, v46\n" \
  "v_exp_f32 v47, v47\n" \
  "v_exp_f32 v16, v16\n" \
  "v_exp_f32 v17, v17\n" \
  "v_smfmac_f32_32x32x32_bf16 v[0:15], v[64:67], v[40:47], v72\n" \
  "v_exp_f32 v18, v18\n" \
  "v_exp_f32 v19, v19\n" \
  "v_exp_f32 v20, v20\n" \
  "v_exp_f32 v21, v21\n" \
  "s_waitcnt lgkmcnt(0)\n" \
  "v_mfma_f32_32x32x16_bf16 v[32:47], v[48:51], v[68:71], v[76:91]\n" \
  "ds_read_b128 v[48:51], v73 offset:8192\n" \
  "ds_read_b128 v[60:63], v74 offset:7392\n" \
  "ds_read_b128 v[64:67], v74 offset:7920\n" \
  "v_exp_f32 v22, v22\n" \
  "v_exp_f32 v23, v23\n" \
  "v_exp_f32 v24, v24\n" \
  "v_exp_f32 v25, v25\n" \
  "v_smfmac_f32_32x32x32_bf16 v[0:15], v[52:55], v[16:23], v72\n" \
  "v_exp_f32 v26, v26\n" \
  "v_exp_f32 v27, v27\n" \
  "v_exp_f32 v28, v28\n" \
  "v_exp_f32 v29, v29\n" \
  "v_exp_f32 v30, v30\n" \
  "v_exp_f32 v31, v31\n" \
  "v_exp_f32 v32, v32\n" \
  "v_exp_f32 v33, v33\n" \
  "v_smfmac_f32_32x32x32_bf16 v[0:15], v[56:59], v[24:31], v72\n" \
  "v_exp_f32 v34, v34\n" \
  "v_exp_f32 v35, v35\n" \
  "v_exp_f32 v36, v36\n" \
  "v_exp_f32 v37, v37\n" \
  "s_waitcnt lgkmcnt(0)\n" \
  "v_mfma_f32_32x32x16_bf16 v[16:31], v[48:51], v[68:71], v[76:91]\n" \
  "ds_read_b128 v[48:51], v73 offset:9216\n" \
  "ds_read_b128 v[52:55], v74 offset:8448\n" \
  "ds_read_b128 v[56:59], v74 offset:8976\n" \
  "v_exp_f32 v38, v38\n" \
  "v_exp_f32 v39, v39\n" \
  "v_exp_f32 v40, v40\n" \
  "v_exp_f32 v41, v41\n" \
  "v_smfmac_f32_32x32x32_bf16 v[0:15], v[60:63], v[32:39], v72\n" \
  "v_exp_f32 v42, v42\n" \
  "v_exp_f32 v43, v43\n" \
  "v_exp_f32 v44, v44\n" \
  "v_exp_f32 v45, v45\n" \
  "v_exp_f32 v46, v46\n" \
  "v_exp_f32 v47, v47\n" \
  "v_add_u32 v73, 0x2000, v73\n" \
  "v_add_u32 v74, 8448, v74\n" \
  "s_sub_u32 %[n], %[n], 1\n" \
  "s_cmp_lg_u32 %[n], 0\n" \
  "s_cbranch_scc1 1b\n" \
  "s_nop 1\n" \
  "v_smfmac_f32_32x32x32_bf16 v[0:15], v[64:67], v[40:47], v72\n" \
  "s_waitcnt lgkmcnt(0)\n" \
  "s_nop 7\n" \
  "s_nop 7\n"

constexpr unsigned WS_EPOCH = 0, WS_TMO = 256;
constexpr unsigned WS_MODC = 4096;
constexpr unsigned WS_MODN = WS_MODC + 16384;
constexpr unsigned WS_LNST = WS_MODN + 16384;
constexpr unsigned WS_PREF = WS_LNST + 32768;
constexpr unsigned WS_KVF  = WS_PREF + 2048;
constexpr unsigned WS_KF   = 262144;
constexpr unsigned WS_VF   = WS_KF + (2u << 20);
constexpr unsigned WS_OF   = WS_VF + (2u << 20);
constexpr unsigned WS_ST   = WS_OF + (2u << 20);
constexpr unsigned WS_CAND = WS_ST + 131072;
constexpr unsigned WS_GU   = WS_CAND + 1024;
constexpr unsigned WS_ROWS = WS_GU + 7168;
constexpr unsigned WS_END  = WS_ROWS + 262144;
constexpr unsigned WS_BYTES = 8u << 20;
static_assert(WS_KVF + 32768 <= WS_KF && WS_END <= WS_BYTES, "ws map");
constexpr unsigned SENT = 0xFFFFFFFFu;
constexpr unsigned VG = 528;
constexpr unsigned L_STAT = 0;
constexpr unsigned L_RED = 384;
constexpr unsigned L_RED2 = 640;
constexpr unsigned L_PST = 896;
constexpr unsigned L_ERED = 1408;
constexpr unsigned L_MODW = 2048;
constexpr unsigned L_LN = 4096;
constexpr unsigned L_QS = 6144;
constexpr unsigned L_MODN = 14336;
constexpr unsigned L_TOK = 22528;
constexpr unsigned L_SEL = 23040;
constexpr unsigned L_K = 25600, L_V = L_K + 65536, L_TOTAL = L_V + 130 * VG;
constexpr unsigned L_WP = L_K + 8192;
constexpr unsigned L_WRAW = (L_V + 16 * VG + 127) / 128 * 128;
constexpr unsigned L_OST = L_K;
constexpr unsigned L_XCH = L_K + 32768;
static_assert(L_TOTAL <= 163840 && L_WRAW + 49152 <= L_TOTAL, "lds");

constexpr unsigned SPIN_CAP = 1u << 17;
constexpr float QSCALE = 0.25f * 1.4426950408889634f;

#define STAMP(i) do {} while (0)
#define STAMPC(i) do {} while (0)
#define STAMP_DECL
#define STAMP_FLUSH(n)
#define SB() __builtin_amdgcn_sched_barrier(0)
#define PIN(x) asm volatile("" : "+v"(x))

struct Args {
  const u16* x; const u16* wqkv; const u16* wo; const u16* wr; const u16* wg; const u16* wu; const u16* wd; const u16* c; const u16* wm;
  u16* out; char* ws;
};
typedef __amdgpu_buffer_rsrc_t rsrc_t;
DEV rsrc_t mkrs(const void* p, unsigned bytes) { return __builtin_amdgcn_make_buffer_rsrc((void*)p, 0, bytes, 0x00020000); }
DEV float lo2f(unsigned v) { return __uint_as_float(v << 16); }
DEV float hi2f(unsigned v) { return __uint_as_float(v & 0xffff0000u); }
DEV unsigned cvtpk(float lo, float hi) { const f32x2 f = {lo, hi}; return __builtin_bit_cast(unsigned, __builtin_convertvector(f, bf16x2)); }
DEV float rbf(float f) { return __uint_as_float(cvtpk(f, f) << 16); }
DEV unsigned nosent(unsigned v) { return v < 0xFFFFFFFEu ? v : 0xFFFFFFFEu; }
DEV f32x16 mfma32(v4u a, v4u b, f32x16 c) { return __builtin_amdgcn_mfma_f32_32x32x16_bf16(__builtin_bit_cast(bf16x8, a), __builtin_bit_cast(bf16x8, b), c, 0, 0, 0); }
DEV f32x4 mfma16(v4u a, v4u b, f32x4 c) { return __builtin_amdgcn_mfma_f32_16x16x32_bf16(__builtin_bit_cast(bf16x8, a), __builtin_bit_cast(bf16x8, b), c, 0, 0, 0); }
DEV float dot2(unsigned a, unsigned b, float c) { return __builtin_amdgcn_fdot2_f32_bf16(__builtin_bit_cast(bf16x2, a), __builtin_bit_cast(bf16x2, b), c, false); }
DEV LAS char* lp(unsigned base) { return (LAS char*)(size_t)base; }
#define LR16(base, off) (*(LAS v4u*)(lp(base) + (off)))
#define LR8(base, off) (*(LAS v2u*)(lp(base) + (off)))
#define LRF4(base, off) (*(LAS f32x4*)(lp(base) + (off)))
#define LRF(base, off) (*(LAS float*)(lp(base) + (off)))
DEV v2u lds_tr(unsigned base, unsigned off) { return __builtin_bit_cast(v2u, __builtin_amdgcn_ds_read_tr16_b64_v4i16((LAS s16x4*)(lp(base) + off))); }
DEV void lds_addf(unsigned base, unsigned off, float v) { __hip_atomic_fetch_add((LAS float*)(lp(base) + off), v, __ATOMIC_RELAXED, __HIP_MEMORY_SCOPE_WORKGROUP); }
DEV v4u bld16(rsrc_t r, unsigned v, unsigned s) { return __builtin_amdgcn_raw_buffer_load_b128(r, v, s, 0); }
DEV v2u bld8(rsrc_t r, unsigned v, unsigned s) { return __builtin_amdgcn_raw_buffer_load_b64(r, v, s, 0); }
DEV v2u ld8_sc1(rsrc_t r, unsigned v, unsigned s) { return __builtin_amdgcn_raw_buffer_load_b64(r, v, s, 16); }
DEV v4u ld16_sc1(rsrc_t r, unsigned v, unsigned s) { return __builtin_amdgcn_raw_buffer_load_b128(r, v, s, 16); }
DEV void st4_sc1(rsrc_t r, unsigned v, unsigned s, unsigned x) { __builtin_amdgcn_raw_buffer_store_b32(x, r, v, s, 16); }
DEV void st8_sc1(rsrc_t r, unsigned v, unsigned s, v2u x) { __builtin_amdgcn_raw_buffer_store_b64(x, r, v, s, 16); }
DEV void st16_sc1(rsrc_t r, unsigned v, unsigned s, v4u x) { __builtin_amdgcn_raw_buffer_store_b128(x, r, v, s, 16); }
DEV v2u granule(unsigned t32, unsigned t8, float v) { return v2u{(__float_as_uint(v) & 0xffffff00u) | t8, t32}; }
DEV unsigned gran_bad(unsigned lo, unsigned hi, unsigned t32, unsigned t8) { return (hi ^ t32) | ((lo ^ t8) & 0xffu); }
DEV unsigned umax4(v4u v) { const unsigned a = v[0] > v[1] ? v[0] : v[1], b = v[2] > v[3] ? v[2] : v[3]; return a > b ? a : b; }
template <int CTRL> DEV float dpp_add(float v) { return v + __builtin_bit_cast(float, __builtin_amdgcn_update_dpp(0, __builtin_bit_cast(int, v), CTRL, 0xf, 0xf, true)); }
template <int CTRL> DEV unsigned dpp_umax(unsigned v) { const unsigned o = (unsigned)__builtin_amdgcn_update_dpp(0, (int)v, CTRL, 0xf, 0xf, true); return o > v ? o : v; }
DEV float swap16_add(float v) { auto r = __builtin_amdgcn_permlane16_swap(__float_as_uint(v), __float_as_uint(v), false, false); return __uint_as_float(r[0]) + __uint_as_float(r[1]); }
DEV float swap32_add(float v) { auto r = __builtin_amdgcn_permlane32_swap(__float_as_uint(v), __float_as_uint(v), false, false); return __uint_as_float(r[0]) + __uint_as_float(r[1]); }
DEV float swap32_max(float v) { auto r = __builtin_amdgcn_permlane32_swap(__float_as_uint(v), __float_as_uint(v), false, false); return fmaxf(__uint_as_float(r[0]), __uint_as_float(r[1])); }
DEV unsigned swap16_umax(unsigned v) { auto r = __builtin_amdgcn_permlane16_swap(v, v, false, false); return r[0] > r[1] ? r[0] : r[1]; }
DEV unsigned swap32_umax(unsigned v) { auto r = __builtin_amdgcn_permlane32_swap(v, v, false, false); return r[0] > r[1] ? r[0] : r[1]; }
DEV float wave_sum(float v) { v = dpp_add<0xB1>(v); v = dpp_add<0x4E>(v); v = dpp_add<0x141>(v); v = dpp_add<0x140>(v); v = swap16_add(v); return swap32_add(v); }
DEV unsigned half_umax(unsigned v) { v = dpp_umax<0xB1>(v); v = dpp_umax<0x4E>(v); v = dpp_umax<0x141>(v); v = dpp_umax<0x140>(v); return swap16_umax(v); }
DEV float frcp(float x) { return __builtin_amdgcn_rcpf(x); }
DEV float sigm(float x) { return frcp(1.f + __builtin_amdgcn_exp2f(-1.4426950408889634f * x)); }
DEV bool spin_fail(unsigned& spins, rsrc_t ws) {
  __builtin_amdgcn_s_sleep(1);
  if (++spins > SPIN_CAP) { st4_sc1(ws, WS_TMO, 0, 1u); return true; }
  return false;
}
DEV unsigned mkkey(float v, unsigned tok) { const unsigned u = __float_as_uint(v); const unsigned o = u ^ ((unsigned)((int)u >> 31) | 0x80000000u); return nosent((o & 0xfffff800u) | (2047u - tok)); }
DEV float keyval(unsigned k) { const unsigned o = k & 0xfffff800u; return __uint_as_float((o & 0x80000000u) ? (o ^ 0x80000000u) : ~o); }

__global__ __launch_bounds__(512, 2) void mega(Args a) {
  extern __shared__ __attribute__((aligned(16))) char lds_[];
  const int tid = threadIdx.x;
  const unsigned l = tid & 63, l16 = l * 16;
  const unsigned w = __builtin_amdgcn_readfirstlane(tid >> 6);
  const unsigned b = blockIdx.x, xcd = b & 7, rr = b >> 3;
  const unsigned qb = (xcd & 3) * 2 + (rr & 1), h = (xcd >> 2) * 16 + (rr >> 1);
  STAMP_DECL;
  STAMP(0);
  const rsrc_t ws = mkrs(a.ws, WS_BYTES), rx = mkrs(a.x, 2u << 20), rw = mkrs(a.wqkv, 1536u * 1024u), rm = mkrs(a.wm, 3u << 20), rc = mkrs(a.c, 1024);
  const u64 e0 = *(const GAS u64*)(a.ws + WS_EPOCH);
  unsigned lds0 = (unsigned)(size_t)(LAS char*)lds_;
  unsigned v16 = lds0 + l16;
  PIN(v16);
  *(LAS v4u*)(lp(lds0 + tid * 16)) = v4u{0u, 0u, 0u, 0u};
  __syncthreads();
  const bool modp = b < 32;
  v4u mw[4]; v2u mc;
  if (modp) {
    const unsigned vo = (tid >> 3) * (4 * 6144) + (tid & 7) * 16, so = (b & 1) * (256 * 6144) + (b >> 1) * 128;
#pragma unroll
    for (int i = 0; i < 4; i++) mw[i] = bld16(rm, vo, so + i * 6144);
    mc = bld8(rc, (tid >> 3) * 8, (b & 1) * 512);
  }
  SB();
  v4u wl[3][2];
  const unsigned vW = (l >> 1) * 3072 + (l & 1) * 16, sW = w * (64 * 3072) + h * 32;
#pragma unroll
  for (int m = 0; m < 2; m++)
#pragma unroll
    for (int i = 0; i < 2; i++) wl[m][i] = bld16(rw, vW, sW + i * (32 * 3072) + m * 1024);
  SB();
  const unsigned dtt = xcd * 8 + (rr >> 2), dcs = rr & 3;
  const bool erole = b < 112, frole = b >= 112 && b < 144;
  {
    const v4u sv = {SENT, SENT, SENT, SENT};
    st16_sc1(ws, l16, WS_OF + ((qb * 8 + w) * 32 + h) * 1024, sv);
    if (w == 4) st16_sc1(ws, l < 32 ? l16 : 0x80000000u, WS_ST + (dtt * 4 + dcs) * 512, sv);
    if (w == 5) st16_sc1(ws, (l >> 4) * 1024 + (l & 15) * 16, WS_ROWS + dtt * 4096 + dcs * 256, sv);
    if (w == 6 && dcs == 0) st8_sc1(ws, l == 0 ? 0u : 0x80000000u, WS_CAND + dtt * 8, v2u{SENT, SENT});
    if (w == 7 && erole) st16_sc1(ws, l < 4 ? (l >> 1) * 3584 + (l & 1) * 16 : 0x80000000u, WS_GU + b * 32, sv);
  }
  const u64 ep = e0 + 0x9E3779B97F4A7C15ull;
  const unsigned t32 = (unsigned)(ep >> 8), t8 = (unsigned)ep & 0xffu;
  STAMP(25);
  if (modp) {
    unsigned sp[4];
#pragma unroll
    for (int i = 0; i < 4; i++) { const float cv = (i & 1) ? hi2f(mc[i >> 1]) : lo2f(mc[i >> 1]); sp[i] = cvtpk(cv * sigm(cv), 0.f); }
    float acc[8];
#pragma unroll
    for (int j = 0; j < 4; j++) {
      acc[2 * j] = dot2(mw[0][j], sp[0], 0.f); acc[2 * j + 1] = dot2(mw[0][j], sp[0] << 16, 0.f);
#pragma unroll
      for (int i = 1; i < 4; i++) { acc[2 * j] = dot2(mw[i][j], sp[i], acc[2 * j]); acc[2 * j + 1] = dot2(mw[i][j], sp[i] << 16, acc[2 * j + 1]); }
    }
#pragma unroll
    for (int j = 0; j < 8; j++) acc[j] = swap32_add(swap16_add(dpp_add<0x128>(acc[j])));
    if (l < 8) {
#pragma unroll
      for (int j = 0; j < 8; j++) lds_addf(lds0 + l * 4, L_RED + j * 32, acc[j]);
    }
    __syncthreads();
    if (w == 0) st8_sc1(ws, l * 8, WS_MODC + ((b & 1) * 1024 + (b >> 1) * 64) * 8, granule(t32, t8, LRF(lds0 + (l & 7) * 32 + (l >> 3) * 4, L_RED)));
  }
  SB();
  STAMP(26);
  const unsigned vX = (l >> 3) * 1024 + (l & 7) * 16, sX = (256 * qb + 32 * w) * 1024;
  v4u xl[8][4];
#define XLOAD(p) { _Pragma("unroll") for (int q = 0; q < 4; q++) xl[p][q] = __builtin_amdgcn_raw_buffer_load_b128(rx, vX + (p) * 128, sX + q * 8192, 0); SB(); }
#pragma unroll
  for (int i = 0; i < 2; i++) wl[2][i] = bld16(rw, vW, sW + i * (32 * 3072) + 2048);
  SB();
  XLOAD(0);
  STAMP(27);
  v2u mg[4];
  const unsigned smg = WS_MODC + w * 512;
#pragma unroll
  for (int i = 0; i < 4; i++) mg[i] = ld8_sc1(ws, l * 8, smg + (i & 1) * 8192 + (i >> 1) * 4096);
  SB();
  XLOAD(1);
  const v4u xs = bld16(rx, l16, (8 * b + w) * 1024);
  SB();
  STAMP(1);
  unsigned wrb = lds0 + L_WRAW + w * 6144 + l16;
  PIN(wrb);
#pragma unroll
  for (int m = 0; m < 3; m++)
#pragma unroll
    for (int i = 0; i < 2; i++) LR16(wrb, m * 2048 + i * 1024) = wl[m][i];
  v4u wraw[3][2];
  {
    unsigned trb = lds0 + L_WRAW + w * 6144 + (8 * (l >> 4) + ((l & 15) >> 2)) * 32 + (l & 3) * 8;
    PIN(trb);
#pragma unroll
    for (int m = 0; m < 3; m++)
#pragma unroll
      for (int s = 0; s < 2; s++) {
        const v2u t0 = lds_tr(trb, m * 2048 + s * 1024), t1 = lds_tr(trb, m * 2048 + s * 1024 + 128);
        wraw[m][s] = v4u{t0[0], t0[1], t1[0], t1[1]};
      }
  }
  STAMP(2);
  v4u xf[2][16];
  unsigned stw0 = lds0 + L_WRAW + w * 6144 + (l >> 3) * 128 + (((l & 7) ^ (l >> 4)) * 16), stw1 = stw0 ^ 64;
  unsigned str0 = lds0 + L_WRAW + w * 6144 + (l & 15) * 128 + (((l >> 4) ^ (((l & 15) >> 1) & 7)) * 16), str1 = str0 ^ 64;
  PIN(stw0); PIN(stw1); PIN(str0); PIN(str1);
#define XPIECE(p)                                                                              \
  {                                                                                            \
    _Pragma("unroll") for (int q = 0; q < 4; q++) LR16((q & 1) ? stw1 : stw0, q * 1024) = xl[p][q]; \
    _Pragma("unroll") for (int u = 0; u < 2; u++) { xf[u][2 * (p)] = LR16(str0, u * 2048); xf[u][2 * (p) + 1] = LR16(str1, u * 2048); } \
  }
  XPIECE(0);
  SB();
  STAMP(3);
  {
    unsigned bad = 0;
#pragma unroll
    for (int i = 0; i < 4; i++) bad |= gran_bad(mg[i][0], mg[i][1], t32, t8);
    if (__any(bad != 0)) {
      unsigned spins = 0;
      do {
        bad = 0;
#pragma unroll
        for (int i = 0; i < 4; i++) { mg[i] = ld8_sc1(ws, l * 8, smg + (i & 1) * 8192 + (i >> 1) * 4096); }
#pragma unroll
        for (int i = 0; i < 4; i++) bad |= gran_bad(mg[i][0], mg[i][1], t32, t8);
      } while (__any(bad != 0) && !spin_fail(spins, ws));
    }
  }
  SB();
  XLOAD(2);
  STAMP(4);
  {
    const float sa = __uint_as_float(mg[0][0]) + __uint_as_float(mg[1][0]);
    const float ca = rbf(__uint_as_float(mg[2][0]) + __uint_as_float(mg[3][0]));
    const unsigned pk = cvtpk(1.f + ca, sa);
    unsigned mwb = lds0 + L_MODW + w * 256;
    *(LAS u16*)(lp(mwb + l * 2)) = (u16)pk;
    *(LAS u16*)(lp(mwb + l * 2) + 128) = (u16)(pk >> 16);
    const unsigned rho = l & 15, kg = l >> 4;
    const unsigned amask = kg == (rho >> 2) ? 0xffffu << (16 * (rho & 1)) : 0u;
    const unsigned m0 = (rho & 2) ? 0u : amask, m1 = (rho & 2) ? amask : 0u;
    unsigned ca1 = mwb + (8 * kg + (rho & 3)) * 2;
    const unsigned one = rho == 0 ? 0x3F803F80u : 0u, srow = rho == 1 ? 0xffffffffu : 0u;
    const v4u Aone = {one, one, one, one};
    unsigned wpb = v16 + L_WP + w * 6144;
    PIN(wpb);
    f32x4 st[3];
#pragma unroll
    for (int s = 0; s < 2; s++) {
      unsigned v1 = *(LAS u16*)(lp(ca1) + 64 * s), v2 = *(LAS u16*)(lp(ca1) + 64 * s + 8);
      v1 |= v1 << 16; v2 |= v2 << 16;
      const v4u A1 = {v1 & m0, v1 & m1, 0u, 0u}, A2 = {0u, 0u, v2 & m0, v2 & m1};
      v4u As = LR16(mwb + kg * 16, 128 + 64 * s);
      As = v4u{As[0] & srow, As[1] & srow, As[2] & srow, As[3] & srow};
#pragma unroll
      for (int m = 0; m < 3; m++) {
        const f32x4 z = {0, 0, 0, 0};
        const f32x4 D1 = mfma16(A1, wraw[m][s], z), D2 = mfma16(A2, wraw[m][s], z);
        const v4u wp = {cvtpk(D1[0], D1[1]), cvtpk(D1[2], D1[3]), cvtpk(D2[0], D2[1]), cvtpk(D2[2], D2[3])};
        LR16(wpb, (s * 3 + m) * 1024) = wp;
        st[m] = mfma16(As, wraw[m][s], s ? st[m] : z);
        st[m] = mfma16(Aone, wp, st[m]);
      }
    }
    if (l < 16) {
#pragma unroll
      for (int m = 0; m < 3; m++) { lds_addf(lds0 + l * 4, L_STAT + m * 128, st[m][0]); lds_addf(lds0 + l * 4, L_STAT + m * 128 + 64, st[m][1]); }
    }
  }
  STAMP(5);
  SB();
  XLOAD(3);
  XPIECE(1);
  {
    float s = dot2(xs[0], 0x3F803F80u, 0.f), q = dot2(xs[0], xs[0], 0.f);
#pragma unroll
    for (int j = 1; j < 4; j++) { s = dot2(xs[j], 0x3F803F80u, s); q = dot2(xs[j], xs[j], q); }
    const float mean = wave_sum(s) * (1.f / 512.f);
    const float var = fmaxf(wave_sum(q) * (1.f / 512.f) - mean * mean, 0.f);
    const float sd = rbf(__builtin_amdgcn_sqrtf(rbf(rbf(var) + 1e-5f)));
    st8_sc1(ws, l < 2 ? l * 8 : 0x80000000u, WS_LNST + (8 * b + w) * 16, granule(t32, t8, l ? sd : rbf(mean)));
  }
  if (w == 0) LR16(lds0 + L_V + (l & 15) * VG, 512) = v4u{0x3F803F80u, 0x3F803F80u, 0x3F803F80u, 0x3F803F80u};
  __syncthreads();
  if (tid == 0) st8_sc1(ws, 0, WS_PREF + b * 8, v2u{(unsigned)ep, (unsigned)(ep >> 32)});
  STAMP(6);
  f32x4 aq[2], ak[2], av[2];
  unsigned wpr = v16 + L_WP;
  PIN(wpr);
#define GSTEP(s)                                                                        \
  {                                                                                     \
    const v4u wq = LR16(wpr, ((s) * 3 + 0) * 1024), wk = LR16(wpr, ((s) * 3 + 1) * 1024), wv = LR16(wpr, ((s) * 3 + 2) * 1024); \
    _Pragma("unroll") for (int u = 0; u < 2; u++) {                                     \
      const f32x4 z = {0, 0, 0, 0};                                                     \
      aq[u] = mfma16(wq, xf[u][s], (s) ? aq[u] : z);                                    \
      ak[u] = mfma16(wk, xf[u][s], (s) ? ak[u] : z);                                    \
      av[u] = mfma16(xf[u][s], wv, (s) ? av[u] : z);                                    \
    }                                                                                   \
  }
  XLOAD(4);
  GSTEP(0); GSTEP(1); SB();
  XLOAD(5);
  GSTEP(2); GSTEP(3); SB();
  XLOAD(6);
  XPIECE(2); GSTEP(4); GSTEP(5); SB();
  XLOAD(7);
  XPIECE(3); GSTEP(6); GSTEP(7); SB();
  const unsigned sln = WS_LNST + (256 * qb + 32 * w) * 16;
  v4u lg0 = ld16_sc1(ws, (l & 15) * 16, sln), lg1 = ld16_sc1(ws, (l & 15) * 16, sln + 256);
  SB();
  XPIECE(4); GSTEP(8); GSTEP(9); SB();
  XPIECE(5); GSTEP(10); GSTEP(11); SB();
  XPIECE(6); GSTEP(12); GSTEP(13); SB();
  XPIECE(7); GSTEP(14); GSTEP(15);
  {
    unsigned bad = gran_bad(lg0[0], lg0[1], t32, t8) | gran_bad(lg0[2], lg0[3], t32, t8) | gran_bad(lg1[0], lg1[1], t32, t8) | gran_bad(lg1[2], lg1[3], t32, t8);
    if (__any(bad != 0)) {
      unsigned spins = 0;
      do {
        lg0 = ld16_sc1(ws, (l & 15) * 16, sln); lg1 = ld16_sc1(ws, (l & 15) * 16, sln + 256);
        bad = gran_bad(lg0[0], lg0[1], t32, t8) | gran_bad(lg0[2], lg0[3], t32, t8) | gran_bad(lg1[0], lg1[1], t32, t8) | gran_bad(lg1[2], lg1[3], t32, t8);
      } while (__any(bad != 0) && !spin_fail(spins, ws));
    }
  }
  STAMP(7);
  v4u qfrag;
  {
    const unsigned g = l >> 4, t = l & 15;
    const float mu[2] = {__uint_as_float(lg0[0]), __uint_as_float(lg1[0])};
    const float rs[2] = {frcp(__uint_as_float(lg0[2])), frcp(__uint_as_float(lg1[2]))};
    unsigned lnb = lds0 + L_LN + w * 256;
    *(LAS f32x2*)(lp(lnb + t * 8)) = f32x2{mu[0], rs[0]};
    *(LAS f32x2*)(lp(lnb + t * 8) + 128) = f32x2{mu[1], rs[1]};
    const unsigned sg = lds0 + L_STAT + g * 16, stt = lds0 + L_STAT + t * 4;
    const f32x4 cq = LRF4(sg, 0), bq = LRF4(sg, 64), ck = LRF4(sg, 128), bk = LRF4(sg, 192);
    const float cv = LRF(stt, 256), bv = LRF(stt, 320);
    const unsigned fo = (g >> 1) * 512 + t * 16 + (g & 1) * 8;
    unsigned kt = lds0 + L_K + w * 1024 + fo, qt = lds0 + L_QS + w * 1024 + fo;
    unsigned vt = lds0 + L_V + 2 * w * VG + (g >> 1) * 256 + t * 16 + (g & 1) * 8;
    unsigned lv = lnb + g * 32;
    PIN(kt); PIN(vt);
#pragma unroll
    for (int u = 0; u < 2; u++) {
      const float rq = rs[u] * QSCALE, nm = -mu[u];
      const f32x4 ma = LRF4(lv, u * 128), mb = LRF4(lv, u * 128 + 16);
      float q[4], k[4], v[4];
#pragma unroll
      for (int r = 0; r < 4; r++) {
        q[r] = fmaf(fmaf(nm, cq[r], aq[u][r]), rq, bq[r] * QSCALE);
        k[r] = fmaf(fmaf(nm, ck[r], ak[u][r]), rs[u], bk[r]);
        const float m_ = r < 2 ? ma[2 * r] : mb[2 * r - 4], r_ = r < 2 ? ma[2 * r + 1] : mb[2 * r - 3];
        v[r] = fmaf(fmaf(-m_, cv, av[u][r]), r_, bv);
      }
      LR8(qt, u * 256) = v2u{cvtpk(q[0], q[1]), cvtpk(q[2], q[3])};
      LR8(kt, u * 256) = v2u{cvtpk(k[0], k[1]), cvtpk(k[2], k[3])};
      LR8(vt, u * VG) = v2u{cvtpk(v[0], v[1]), cvtpk(v[2], v[3])};
    }
    qfrag = LR16(v16, L_QS + w * 1024);
    unsigned kp = v16 + L_K + w * 1024, vp = lds0 + L_V + (2 * w + (l >> 5)) * VG + (l & 31) * 16;
    const v4u kpub = LR16(kp, 0), vpub = LR16(vp, 0);
    st16_sc1(ws, l16, WS_KF + (h * 64 + qb * 8 + w) * 1024, kpub);
    st16_sc1(ws, l16, WS_VF + (h * 64 + qb * 8 + w) * 1024, vpub);
  }
  STAMP(8);
  __syncthreads();
  STAMP(9);
  {
    const v4u on = {0x3F803F80u, 0x3F803F80u, 0x3F803F80u, 0x3F803F80u};
    if (w < 2) { const unsigned gi = 16 + (tid & 127); LR16(lds0 + L_V + (gi < 129 ? gi : 129) * VG, 512) = on; }
  }
  f32x16 O, negm, SA, SB_;
#pragma unroll
  for (int i = 0; i < 16; i++) { O[i] = 0.f; negm[i] = 0.f; }
  const unsigned i32 = l & 31, ha = l >> 5;
  const unsigned vrel = i32 < 16 ? ha * 256 + i32 * 16 : (i32 < 20 || i32 >= 28) ? 512u : i32 < 24 ? ha * 256 + (i32 - 20) * 16 : ha * 256 + (i32 - 12) * 16;
  unsigned ka = v16 + L_K, va = lds0 + L_V + vrel, va2 = va + 64 * VG;
  const unsigned idxv = 0xDDDD;
  v4u kf, a0, a1, b0, b1, s0k, s0v, s1k, s1v;
  {
    const v2u epv = {(unsigned)ep, (unsigned)(ep >> 32)};
    const unsigned fo = WS_KVF + (h * 8 + qb) * 128 + w * 8;
    const unsigned vfl = WS_KVF + h * 1024 + (l >> 3) * 128 + (l & 7) * 8;
    v2u fl;
    asm volatile(ATTN_A
      : "+{v[0:15]}"(O), "=&{v[16:31]}"(SA), "=&{v[32:47]}"(SB_), "=&{v[48:51]}"(kf), "=&{v[52:55]}"(a0), "=&{v[56:59]}"(a1), "=&{v[60:63]}"(b0), "=&{v[64:67]}"(b1),
        "=&{v[100:101]}"(fl)
      : "{v[68:71]}"(qfrag), "{v72}"(idxv), "{v73}"(ka), "{v74}"(va), "{v75}"(va2), "{v[76:91]}"(negm), "{v[92:93]}"(epv), "{v99}"(vfl), [ws] "s"(ws), [fo] "s"(fo)
      : "memory");
    STAMP(10);
    if (__any(fl[0] != epv[0] || fl[1] != epv[1])) {
      unsigned spins = 0;
      do { fl = ld8_sc1(ws, vfl, 0); } while (__any(fl[0] != epv[0] || fl[1] != epv[1]) && !spin_fail(spins, ws));
    }
  }
  STAMP(11);
  const unsigned skb = WS_KF + (h * 64 + w) * 1024;
  s1k = ld16_sc1(ws, l16, skb + ((qb + 1) & 7) * 8192); s1v = ld16_sc1(ws, l16, skb + ((qb + 1) & 7) * 8192 + (2u << 20));
  SB();
  s0k = ld16_sc1(ws, l16, skb + ((qb + 2) & 7) * 8192); s0v = ld16_sc1(ws, l16, skb + ((qb + 2) & 7) * 8192 + (2u << 20));
  v4u pf0 = ld16_sc1(ws, l16, WS_PREF + (w & 1) * 1024);
  const bool modn = b >= 144 && b < 176;
  v4u nw[8]; v4u nc;
  if (modn) {
    const unsigned vo = (tid >> 3) * (8 * 6144) + (tid & 7) * 16, so = 2048 + (b - 144) * 128;
#pragma unroll
    for (int i = 0; i < 8; i++) nw[i] = bld16(rm, vo, so + i * 6144);
    nc = bld16(rc, (tid >> 3) * 16, 0);
  }
  SB();
  unsigned kw = v16 + L_K + w * 1024, vw = lds0 + L_V + (2 * w + (l >> 5)) * VG + (l & 31) * 16;
  PIN(kw); PIN(vw);
  LR16(kw, 8192) = s1k; LR16(vw, 16 * VG) = s1v;
  if (__any(pf0[0] != (unsigned)ep || pf0[1] != (unsigned)(ep >> 32) || pf0[2] != (unsigned)ep || pf0[3] != (unsigned)(ep >> 32))) {
    unsigned spins = 0;
    do { pf0 = ld16_sc1(ws, l16, WS_PREF + (w & 1) * 1024); }
    while (__any(pf0[0] != (unsigned)ep || pf0[1] != (unsigned)(ep >> 32) || pf0[2] != (unsigned)ep || pf0[3] != (unsigned)(ep >> 32)) && !spin_fail(spins, ws));
  }
  if (modn) {
    unsigned sp[8];
#pragma unroll
    for (int i = 0; i < 8; i++) { const float cv = (i & 1) ? hi2f(nc[i >> 1]) : lo2f(nc[i >> 1]); sp[i] = cvtpk(cv * sigm(cv), 0.f); }
    float acc[8];
#pragma unroll
    for (int j = 0; j < 4; j++) {
      acc[2 * j] = dot2(nw[0][j], sp[0], 0.f); acc[2 * j + 1] = dot2(nw[0][j], sp[0] << 16, 0.f);
#pragma unroll
      for (int i = 1; i < 8; i++) { acc[2 * j] = dot2(nw[i][j], sp[i], acc[2 * j]); acc[2 * j + 1] = dot2(nw[i][j], sp[i] << 16, acc[2 * j + 1]); }
    }
#pragma unroll
    for (int j = 0; j < 8; j++) acc[j] = swap32_add(swap16_add(dpp_add<0x128>(acc[j])));
    if (l < 8) {
#pragma unroll
      for (int j = 0; j < 8; j++) lds_addf(lds0 + l * 4, L_RED2 + j * 32, acc[j]);
    }
  }
  const unsigned ct = w & 3, kh = w >> 2;
  const unsigned c0 = 128 * dcs + 32 * ct + 16 * kh + 4 * ha;
  const rsrc_t rwr = mkrs(a.wr, 1024);
  const v2u xr0 = bld8(rx, i32 * 1024 + c0 * 2, dtt * 32768), xr1 = bld8(rx, i32 * 1024 + c0 * 2 + 16, dtt * 32768);
  const v2u wr0 = bld8(rwr, c0 * 2, 0), wr1 = bld8(rwr, c0 * 2 + 16, 0);
  const v4u wrall = bld16(rwr, l16, 0);
  __syncthreads();
  if (modn && w == 0) st8_sc1(ws, l * 8, WS_MODN + (b - 144) * 512, granule(t32, t8, rbf(LRF(lds0 + (l & 7) * 32 + (l >> 3) * 4, L_RED2))));
  STAMP(13);
  STAMPC(14);
  v16u wonA, wonB, wonC, wonD;
  {
    const rsrc_t rwo = mkrs(a.wo, 512u * 1024u);
    const rsrc_t refa = erole ? mkrs(a.wg, 512u * 3584u) : mkrs(a.wd, frole ? 1792u * 1024u : 0u);
    const rsrc_t refb = erole ? mkrs(a.wu, 512u * 3584u) : refa;
    const rsrc_t refc = mkrs(a.wd, frole ? 1792u * 1024u : 0u);
    unsigned swo = kh * (256 * 1024) + dcs * 256 + ct * 64;
    const unsigned vwo = (l >> 2) * 1024 + (l & 3) * 16;
    const unsigned estr = erole ? 3584u : 1024u;
    const unsigned vef = (l >> 1) * estr + (l & 1) * 16;
    unsigned sef = erole ? w * (64 * 3584) + b * 32 : w * (224 * 1024) + (b - 112) * 32;
    const unsigned sefs = 32 * estr, sefr = erole ? 64 * 3584 : 0u;
    unsigned sq = (qb + 2) & 7, st_ = 0;
    v16u efA; v4u ef4, ef5, ef6;
    asm volatile(ATTN_B
      : "+{v[0:15]}"(O), "=&{v[16:31]}"(SA), "=&{v[32:47]}"(SB_), "=&{v[48:51]}"(kf), "=&{v[52:55]}"(a0), "=&{v[56:59]}"(a1), "=&{v[60:63]}"(b0), "=&{v[64:67]}"(b1),
        "+{v[100:103]}"(s0k), "+{v[104:107]}"(s0v), "=&{v[108:111]}"(s1k), "=&{v[112:115]}"(s1v),
        "=&{v[128:143]}"(wonA), "=&{v[144:159]}"(wonB), "=&{v[160:175]}"(wonC), "=&{v[176:191]}"(wonD),
        "=&{v[192:207]}"(efA), "=&{v[208:211]}"(ef4), "=&{v[212:215]}"(ef5), "=&{v[216:219]}"(ef6),
        [swo] "+s"(swo), [sef] "+s"(sef), [sq] "+s"(sq), [st] "+s"(st_)
      : "{v[68:71]}"(qfrag), "{v72}"(idxv), "{v73}"(ka), "{v74}"(va), "{v75}"(va2), "{v[76:91]}"(negm), "{v94}"(l16), "{v95}"(kw), "{v96}"(vw), "{v97}"(vwo), "{v98}"(vef),
        [ws] "s"(ws), [rwo] "s"(rwo), [refa] "s"(refa), [refb] "s"(refb), [refc] "s"(refc), [skb] "s"(skb), [sefs] "s"(sefs), [sefr] "s"(sefr)
      : "memory", "scc");
    STAMPC(15);
    STAMP(16);
    {
      float lsum = ha ? O[12] : O[8];
      const float mx = fmaxf(fmaxf(fmaxf(fabsf(O[0]), fabsf(O[1])), fmaxf(fabsf(O[2]), fabsf(O[3]))), fmaxf(fmaxf(fabsf(O[4]), fabsf(O[5])), fmaxf(fabsf(O[6]), fabsf(O[7]))));
      const bool okl = lsum >= 5.421010862427522e-20f && lsum <= 1.8446744073709552e19f && mx <= 3.0e38f;
      if (!__all(okl)) {
        float m = -3.0e38f;
        const f32x16 z = negm;
        for (unsigned kt = 0; kt < 64; kt++) {
          const f32x16 s = mfma32(LR16(ka + kt * 1024, 0), qfrag, z);
#pragma unroll
          for (int i = 0; i < 16; i++) m = fmaxf(m, s[i]);
        }
        m = swap32_max(m);
#pragma unroll
        for (int i = 0; i < 16; i++) { negm[i] = -m; O[i] = 0.f; }
        unsigned ka_ = ka, va_ = va; int n = 8;
        asm volatile(ATTN_F
          : "+{v[0:15]}"(O), "=&{v[16:31]}"(SA), "=&{v[32:47]}"(SB_), "=&{v[48:51]}"(kf), "=&{v[52:55]}"(a0), "=&{v[56:59]}"(a1), "=&{v[60:63]}"(b0), "=&{v[64:67]}"(b1),
            "+{v73}"(ka_), "+{v74}"(va_), [n] "+s"(n)
          : "{v[68:71]}"(qfrag), "{v72}"(idxv), "{v[76:91]}"(negm) : "memory", "scc");
        lsum = ha ? O[12] : O[8];
      }
      const float inv = frcp(lsum);
      const v4u o = {nosent(cvtpk(O[0] * inv, O[1] * inv)), nosent(cvtpk(O[2] * inv, O[3] * inv)), nosent(cvtpk(O[4] * inv, O[5] * inv)), nosent(cvtpk(O[6] * inv, O[7] * inv))};
      st16_sc1(ws, l16, WS_OF + ((qb * 8 + w) * 32 + h) * 1024, o);
    }
    STAMP(17);
    v4u g0 = ld16_sc1(ws, tid * 32, WS_MODN), g1 = ld16_sc1(ws, tid * 32 + 16, WS_MODN);
    SB();
    unsigned priv = v16 + L_QS + w * 1024;
    unsigned tra = lds0 + L_QS + w * 1024 + (4 * ha + ((l & 15) >> 2)) * 64 + ((l >> 4) & 1) * 32 + (l & 3) * 8;
    unsigned trb = lds0 + L_QS + w * 1024 + (8 * (l >> 4) + ((l & 15) >> 2)) * 32 + (l & 3) * 8;
    v4u woA[16], efB[7];
#pragma unroll
    for (int i = 0; i < 16; i++) {
      const v16u& src = i < 4 ? wonA : i < 8 ? wonB : i < 12 ? wonC : wonD;
      LR16(priv, 0) = v4u{src[4 * (i & 3)], src[4 * (i & 3) + 1], src[4 * (i & 3) + 2], src[4 * (i & 3) + 3]};
      const v2u t0 = lds_tr(tra, 0), t1 = lds_tr(tra, 512);
      woA[i] = v4u{t0[0], t0[1], t1[0], t1[1]};
    }
    if (erole || frole) {
#pragma unroll
      for (int i = 0; i < 7; i++) {
        LR16(priv, 0) = i < 4 ? v4u{efA[4 * i], efA[4 * i + 1], efA[4 * i + 2], efA[4 * i + 3]} : i == 4 ? ef4 : i == 5 ? ef5 : ef6;
        const v2u t0 = lds_tr(trb, 0), t1 = lds_tr(trb, 128);
        efB[i] = v4u{t0[0], t0[1], t1[0], t1[1]};
      }
    }
    {
      unsigned bad = gran_bad(g0[0], g0[1], t32, t8) | gran_bad(g0[2], g0[3], t32, t8) | gran_bad(g1[0], g1[1], t32, t8) | gran_bad(g1[2], g1[3], t32, t8);
      if (__any(bad != 0)) {
        unsigned spins = 0;
        do {
          g0 = ld16_sc1(ws, tid * 32, WS_MODN); g1 = ld16_sc1(ws, tid * 32 + 16, WS_MODN);
          bad = gran_bad(g0[0], g0[1], t32, t8) | gran_bad(g0[2], g0[3], t32, t8) | gran_bad(g1[0], g1[1], t32, t8) | gran_bad(g1[2], g1[3], t32, t8);
        } while (__any(bad != 0) && !spin_fail(spins, ws));
      }
      LR16(lds0 + tid * 16, L_MODN) = v4u{g0[0] & 0xffff0000u, g0[2] & 0xffff0000u, g1[0] & 0xffff0000u, g1[2] & 0xffff0000u};
    }
    SB();
    STAMP(18);
    v4u og[4];
    const unsigned sog = WS_OF + (dtt * 32 + 4 * w) * 1024;
#pragma unroll
    for (int i = 0; i < 4; i++) og[i] = ld16_sc1(ws, l16, sog + i * 1024);
    __syncthreads();
    float xv[8], gav[8], c2v[8], sbv[8], gbv[8], vr[8];
    {
      unsigned mb_ = lds0 + L_MODN + c0 * 4;
#pragma unroll
      for (int j = 0; j < 2; j++) {
        const f32x4 ga4 = LRF4(mb_, 32 * j), sb4 = LRF4(mb_, 2048 + 32 * j), cb4 = LRF4(mb_, 4096 + 32 * j), gb4 = LRF4(mb_, 6144 + 32 * j);
        const v2u xr = j ? xr1 : xr0, wr = j ? wr1 : wr0;
#pragma unroll
        for (int r = 0; r < 4; r += 2) {
          const unsigned c2p = cvtpk(1.f + cb4[r], 1.f + cb4[r + 1]);
          c2v[4 * j + r] = lo2f(c2p); c2v[4 * j + r + 1] = hi2f(c2p);
          xv[4 * j + r] = lo2f(xr[r >> 1]); xv[4 * j + r + 1] = hi2f(xr[r >> 1]);
          vr[4 * j + r] = c2v[4 * j + r] * lo2f(wr[r >> 1]); vr[4 * j + r + 1] = c2v[4 * j + r + 1] * hi2f(wr[r >> 1]);
        }
#pragma unroll
        for (int r = 0; r < 4; r++) { gav[4 * j + r] = ga4[r]; sbv[4 * j + r] = sb4[r]; gbv[4 * j + r] = gb4[r]; }
      }
    }
    float cvs = 0.f, cbs = 0.f;
    if (w == 0) {
      const unsigned mb_ = lds0 + L_MODN + l * 32;
      const f32x4 ca_ = LRF4(mb_, 4096), cb_ = LRF4(mb_, 4096 + 16), sa_ = LRF4(mb_, 2048), sb_ = LRF4(mb_, 2048 + 16);
#pragma unroll
      for (int j = 0; j < 8; j += 2) {
        const f32x4& cc = j < 4 ? ca_ : cb_; const f32x4& ss = j < 4 ? sa_ : sb_;
        const unsigned c2p = cvtpk(1.f + cc[j & 3], 1.f + cc[(j & 3) + 1]);
        const float w0 = lo2f(wrall[j >> 1]), w1 = hi2f(wrall[j >> 1]);
        cvs = fmaf(lo2f(c2p), w0, fmaf(hi2f(c2p), w1, cvs));
        cbs = fmaf(ss[j & 3], w0, fmaf(ss[(j & 3) + 1], w1, cbs));
      }
      cvs = wave_sum(cvs); cbs = wave_sum(cbs);
    }
    if (__any(umax4(og[0]) == SENT || umax4(og[1]) == SENT || umax4(og[2]) == SENT || umax4(og[3]) == SENT)) {
      unsigned spins = 0;
      do {
#pragma unroll
        for (int i = 0; i < 4; i++) og[i] = ld16_sc1(ws, l16, sog + i * 1024);
      } while (__any(umax4(og[0]) == SENT || umax4(og[1]) == SENT || umax4(og[2]) == SENT || umax4(og[3]) == SENT) && !spin_fail(spins, ws));
    }
    STAMP(19);
    unsigned ost = v16 + L_OST;
    PIN(ost);
#pragma unroll
    for (int i = 0; i < 4; i++) LR16(ost + w * 4096, i * 1024) = og[i];
    __syncthreads();
    f32x16 da;
    {
      const unsigned osr = ost + kh * 16384;
      const f32x16 z = {0.f};
#pragma unroll
      for (int i = 0; i < 16; i++) da = mfma32(woA[i], LR16(osr, i * 1024), i ? da : z);
    }
    unsigned xch = v16 + L_XCH;
    PIN(xch);
    float own[8];
    if (kh) {
      LRF4(xch + w * 2048, 0) = f32x4{da[0], da[1], da[2], da[3]}; LRF4(xch + w * 2048, 1024) = f32x4{da[4], da[5], da[6], da[7]};
#pragma unroll
      for (int i = 0; i < 8; i++) own[i] = da[8 + i];
    } else {
      LRF4(xch + w * 2048, 0) = f32x4{da[8], da[9], da[10], da[11]}; LRF4(xch + w * 2048, 1024) = f32x4{da[12], da[13], da[14], da[15]};
#pragma unroll
      for (int i = 0; i < 8; i++) own[i] = da[i];
    }
    __syncthreads();
    float x1[8];
    {
      const f32x4 p0 = LRF4(xch + (w ^ 4) * 2048, 0), p1 = LRF4(xch + (w ^ 4) * 2048, 1024);
      float s1 = 0.f, s2 = 0.f, s3 = 0.f;
#pragma unroll
      for (int i = 0; i < 8; i++) {
        const float xx = fmaf(gav[i], own[i] + (i < 4 ? p0[i & 3] : p1[i & 3]), xv[i]);
        x1[i] = xx; s1 += xx; s2 = fmaf(xx, xx, s2); s3 = fmaf(xx, vr[i], s3);
      }
      s1 = swap32_add(s1); s2 = swap32_add(s2); s3 = swap32_add(s3);
      if (l < 32) { const unsigned pa = lds0 + l * 4; lds_addf(pa, L_PST, s1); lds_addf(pa, L_PST + 128, s2); lds_addf(pa, L_PST + 256, s3); }
    }
    __syncthreads();
    STAMP(20);
    if (w == 0) {
      const unsigned pr = lds0 + i32 * 4;
      const unsigned q1 = *(LAS unsigned*)(lp(pr) + L_PST), q2 = *(LAS unsigned*)(lp(pr) + L_PST + 128), q3 = *(LAS unsigned*)(lp(pr) + L_PST + 256);
      st16_sc1(ws, l < 32 ? l16 : 0x80000000u, WS_ST + (dtt * 4 + dcs) * 512, v4u{nosent(q1), nosent(q2), nosent(q3), 0u});
      v4u pa, pb;
      unsigned spins = 0;
      do {
        pa = ld16_sc1(ws, l16, WS_ST + dtt * 2048); pb = ld16_sc1(ws, l16, WS_ST + dtt * 2048 + 1024);
      } while (__any(umax4(pa) == SENT || umax4(pb) == SENT) && !spin_fail(spins, ws));
      const float t1 = swap32_add(__uint_as_float(pa[0]) + __uint_as_float(pb[0]));
      const float t2 = swap32_add(__uint_as_float(pa[1]) + __uint_as_float(pb[1]));
      const float t3 = swap32_add(__uint_as_float(pa[2]) + __uint_as_float(pb[2]));
      const float mean = t1 * (1.f / 512.f);
      const float var = fmaxf(t2 * (1.f / 512.f) - mean * mean, 0.f);
      const float mb = rbf(mean), rs = frcp(rbf(__builtin_amdgcn_sqrtf(rbf(rbf(var) + 1e-5f))));
      const float rrv = fmaf(t3 - mb * cvs, rs, cbs);
      const unsigned key = mkkey(rrv, 32 * dtt + i32);
      const unsigned k1 = half_umax(key), k2 = half_umax(key == k1 ? 0u : key);
      if (dcs == 0) st8_sc1(ws, l == 0 ? 0u : 0x80000000u, WS_CAND + dtt * 8, v2u{k1, k2});
      *(LAS f32x2*)(lp(lds0 + i32 * 16) + L_TOK) = f32x2{rs, -mb * rs};
      *(LAS v2u*)(lp(lds0) + L_SEL) = v2u{(2047u - (k1 & 2047u)) & 31u, (2047u - (k2 & 2047u)) & 31u};
    }
    __syncthreads();
    STAMP(21);
    const v2u loc = LR8(lds0, L_SEL);
    v2u o0, o1;
    const bool held = i32 == loc[0] || i32 == loc[1];
    const rsrc_t ro = mkrs(a.out, 2u << 20);
    const unsigned voo = i32 * 1024 + c0 * 2, soo = dtt * 32768;
    {
      const f32x2 tk = *(LAS f32x2*)(lp(lds0 + i32 * 16) + L_TOK);
      float h2[8], ot[8];
#pragma unroll
      for (int j = 0; j < 8; j++) { h2[j] = fmaf(fmaf(x1[j], tk[0], tk[1]), c2v[j], sbv[j]); ot[j] = fmaf(gbv[j], h2[j], x1[j]); }
      o0 = v2u{cvtpk(ot[0], ot[1]), cvtpk(ot[2], ot[3])}; o1 = v2u{cvtpk(ot[4], ot[5]), cvtpk(ot[6], ot[7])};
      if (held) {
        const unsigned ro_ = WS_ROWS + dtt * 4096 + (i32 == loc[0] ? 0u : 2048u) + c0 * 2;
        st8_sc1(ws, ro_, 0, v2u{nosent(cvtpk(h2[0], h2[1])), nosent(cvtpk(h2[2], h2[3]))});
        st8_sc1(ws, ro_ + 16, 0, v2u{nosent(cvtpk(h2[4], h2[5])), nosent(cvtpk(h2[6], h2[7]))});
        st8_sc1(ws, ro_ + 1024, 0, v2u{nosent(cvtpk(x1[0], x1[1])), nosent(cvtpk(x1[2], x1[3]))});
        st8_sc1(ws, ro_ + 1040, 0, v2u{nosent(cvtpk(x1[4], x1[5])), nosent(cvtpk(x1[6], x1[7]))});
      }
      const unsigned vo_ = held ? 0x80000000u : voo;
      __builtin_amdgcn_raw_buffer_store_b64(o0, ro, vo_, soo, 0);
      __builtin_amdgcn_raw_buffer_store_b64(o1, ro, vo_ + 16, soo, 0);
    }
    STAMP(22);
    if (w == 0) {
      v2u cd;
      unsigned spins = 0;
      do { cd = ld8_sc1(ws, l * 8, WS_CAND); } while (__any(cd[0] == SENT || cd[1] == SENT) && !spin_fail(spins, ws));
      const unsigned K1 = swap32_umax(half_umax(cd[0]));
      const unsigned K2 = swap32_umax(half_umax(cd[0] == K1 ? cd[1] : cd[0]));
      const bool second = __any(cd[0] == K1 && cd[1] == K2);
      const unsigned T1 = 2047u - (K1 & 2047u), T2 = 2047u - (K2 & 2047u);
      *(LAS v4u*)(lp(lds0) + L_SEL + 16) = v4u{T1, T2, WS_ROWS + (T1 >> 5) * 4096, WS_ROWS + (T2 >> 5) * 4096 + (second ? 2048u : 0u)};
      *(LAS f32x2*)(lp(lds0) + L_SEL + 32) = f32x2{keyval(K1), keyval(K2)};
    }
    __syncthreads();
    STAMP(23);
    const v4u sel = LR16(lds0, L_SEL + 16);
    if (held) {
      const unsigned tk = 32 * dtt + i32;
      const unsigned vo_ = (tk == sel[0] || tk == sel[1]) ? 0x80000000u : voo;
      __builtin_amdgcn_raw_buffer_store_b64(o0, ro, vo_, soo, 0);
      __builtin_amdgcn_raw_buffer_store_b64(o1, ro, vo_ + 16, soo, 0);
    }
    const unsigned rw_ = l & 15, kg = l >> 4;
    if (erole) {
      const unsigned vxa = rw_ < 2 ? (rw_ ? sel[3] : sel[2]) + kg * 16 : 0x80000000u;
      v4u xa0, xa1;
      unsigned spins = 0;
      do { xa0 = ld16_sc1(ws, vxa, w * 128); xa1 = ld16_sc1(ws, vxa, w * 128 + 64); } while (__any(umax4(xa0) == SENT || umax4(xa1) == SENT) && !spin_fail(spins, ws));
      const f32x4 z = {0.f, 0.f, 0.f, 0.f};
      const f32x4 dg = mfma16(xa1, efB[1], mfma16(xa0, efB[0], z)), du = mfma16(xa1, efB[3], mfma16(xa0, efB[2], z));
      if (l < 16) { lds_addf(lds0 + l * 4, L_ERED, dg[0]); lds_addf(lds0 + l * 4, L_ERED + 64, dg[1]); lds_addf(lds0 + l * 4, L_ERED + 128, du[0]); lds_addf(lds0 + l * 4, L_ERED + 192, du[1]); }
      __syncthreads();
      if (w == 0) {
        const unsigned ea = lds0 + rw_ * 4;
        const float ga0 = rbf(LRF(ea, L_ERED)), ga1 = rbf(LRF(ea, L_ERED + 64));
        const float gu0 = rbf(ga0 * sigm(ga0)) * rbf(LRF(ea, L_ERED + 128)), gu1 = rbf(ga1 * sigm(ga1)) * rbf(LRF(ea, L_ERED + 192));
        const float n0 = __builtin_bit_cast(float, __builtin_amdgcn_update_dpp(0, __builtin_bit_cast(int, gu0), 0xB1, 0xf, 0xf, true));
        const float n1 = __builtin_bit_cast(float, __builtin_amdgcn_update_dpp(0, __builtin_bit_cast(int, gu1), 0xB1, 0xf, 0xf, true));
        const unsigned pk = (l & 1) ? cvtpk(n1, gu1) : cvtpk(gu0, n0);
        st4_sc1(ws, l < 16 ? (l & 1) * 3584 + (l >> 1) * 4 : 0x80000000u, WS_GU + b * 32, nosent(pk));
      }
    } else if (frole) {
      const unsigned fc = (b - 112) * 16 + rw_, ft = kg & 1;
      const unsigned vga = rw_ < 2 ? rw_ * 3584 + kg * 16 : 0x80000000u;
      const unsigned vhx = (ft ? sel[3] : sel[2]) + (fc & ~1u) * 2;
      v4u ga_[7]; unsigned hx, xx;
      unsigned spins = 0; bool bad;
      do {
#pragma unroll
        for (int s = 0; s < 7; s++) ga_[s] = ld16_sc1(ws, vga, WS_GU + w * 448 + s * 64);
        hx = __builtin_amdgcn_raw_buffer_load_b32(ws, vhx, 0, 16); xx = __builtin_amdgcn_raw_buffer_load_b32(ws, vhx, 1024, 16);
        unsigned mx = hx > xx ? hx : xx;
#pragma unroll
        for (int s = 0; s < 7; s++) { const unsigned t = umax4(ga_[s]); mx = t > mx ? t : mx; }
        bad = __any(mx == SENT);
      } while (bad && !spin_fail(spins, ws));
      f32x4 dy = {0.f, 0.f, 0.f, 0.f};
#pragma unroll
      for (int s = 0; s < 7; s++) dy = mfma16(ga_[s], efB[s], dy);
      if (l < 16) { lds_addf(lds0 + l * 4, L_ERED, dy[0]); lds_addf(lds0 + l * 4, L_ERED + 64, dy[1]); }
      __syncthreads();
      if (w == 0) {
        const float y = LRF(lds0 + rw_ * 4 + ft * 64, L_ERED);
        const float sg = rbf(sigm(rbf(LRF(lds0 + ft * 4, L_SEL + 32))));
        const float upd = rbf(rbf(y) * sg);
        const float hv = (fc & 1) ? hi2f(hx) : lo2f(hx), xv_ = (fc & 1) ? hi2f(xx) : lo2f(xx);
        const float res = fmaf(LRF(lds0 + fc * 4, L_MODN + 6144), rbf(hv + upd), xv_);
        __builtin_amdgcn_raw_buffer_store_b16((u16)(cvtpk(res, res) & 0xffffu), ro, l < 32 ? (ft ? sel[1] : sel[0]) * 1024 + fc * 2 : 0x80000000u, 0, 0);
        if (b == 112 && l == 0) *(GAS u64*)(a.ws + WS_EPOCH) = ep;
      }
    }
    STAMP(24);
    STAMP_FLUSH(28);

  }
}

extern "C" void kernel_launch(void* const* d_in, const int* in_sizes, int n_in, void* d_out, int out_size, void* d_ws, size_t ws_size, hipStream_t stream) {
  Args a;
  a.x = (const u16*)d_in[0]; a.wqkv = (const u16*)d_in[1]; a.wo = (const u16*)d_in[2]; a.wr = (const u16*)d_in[4];
  a.wg = (const u16*)d_in[5]; a.wu = (const u16*)d_in[6]; a.wd = (const u16*)d_in[7]; a.c = (const u16*)d_in[8]; a.wm = (const u16*)d_in[9];
  a.out = (u16*)d_out; a.ws = (char*)d_ws;
  (void)hipFuncSetAttribute((const void*)mega, hipFuncAttributeMaxDynamicSharedMemorySize, L_TOTAL);
  mega<<<256, 512, L_TOTAL, stream>>>(a);
}
```

```cpp
#include <hip/hip_runtime.h>
#include <hip/hip_bf16.h>
#include <stdint.h>
typedef unsigned short u16;
typedef unsigned long long u64;
typedef float f32x2 __attribute__((ext_vector_type(2)));
typedef float f32x4 __attribute__((ext_vector_type(4)));
typedef float f32x16 __attribute__((ext_vector_type(16)));
typedef __bf16 bf16x2 __attribute__((ext_vector_type(2)));
typedef __bf16 bf16x8 __attribute__((ext_vector_type(8)));
typedef unsigned v4u __attribute__((ext_vector_type(4)));
typedef unsigned v2u __attribute__((ext_vector_type(2)));
typedef unsigned v16u __attribute__((ext_vector_type(16)));
typedef unsigned v32u __attribute__((ext_vector_type(32)));
typedef short s16x4 __attribute__((ext_vector_type(4)));
#define LAS __attribute__((address_space(3)))
#define GAS __attribute__((address_space(1)))
#define DEV __device__ __forceinline__

#define ATTN_A \
  "ds_read_b128 v[48:51], v73 offset:0\n" \
  "v_mov_b32 v52, v91\n" \
  "v_mov_b32 v53, v91\n" \
  "v_mov_b32 v54, v91\n" \
  "v_mov_b32 v55, v91\n" \
  "v_mov_b32 v56, v91\n" \
  "v_mov_b32 v57, v91\n" \
  "v_mov_b32 v58, v91\n" \
  "v_mov_b32 v59, v91\n" \
  "v_mov_b32 v60, v91\n" \
  "v_mov_b32 v61, v91\n" \
  "v_mov_b32 v62, v91\n" \
  "v_mov_b32 v63, v91\n" \
  "v_mov_b32 v64, v91\n" \
  "v_mov_b32 v65, v91\n" \
  "v_mov_b32 v66, v91\n" \
  "v_mov_b32 v67, v91\n" \
  "s_mov_b64 exec, %[vm]\n" \
  "ds_read_b128 v[52:55], v74 offset:0\n" \
  "ds_read_b128 v[56:59], v74 offset:512\n" \
  "s_mov_b64 exec, -1\n" \
  "v_mov_b32 v0, 0\n" \
  "v_mov_b32 v1, 0\n" \
  "v_mov_b32 v2, 0\n" \
  "v_mov_b32 v3, 0\n" \
  "v_mov_b32 v4, 0\n" \
  "v_mov_b32 v5, 0\n" \
  "v_mov_b32 v6, 0\n" \
  "v_mov_b32 v7, 0\n" \
  "v_mov_b32 v8, 0\n" \
  "v_mov_b32 v9, 0\n" \
  "v_mov_b32 v10, 0\n" \
  "v_mov_b32 v11, 0\n" \
  "v_mov_b32 v12, 0\n" \
  "v_mov_b32 v13, 0\n" \
  "v_mov_b32 v14, 0\n" \
  "v_mov_b32 v15, 0\n" \
  "s_waitcnt lgkmcnt(2)\n" \
  "v_mfma_f32_32x32x16_bf16 v[16:31], v[48:51], v[68:71], 0\n" \
  "ds_read_b128 v[48:51], v73 offset:1024\n" \
  "v_mov_b32 v40, 0\n" \
  "v_mov_b32 v41, 0\n" \
  "v_mov_b32 v42, 0\n" \
  "v_mov_b32 v43, 0\n" \
  "v_mov_b32 v44, 0\n" \
  "v_mov_b32 v45, 0\n" \
  "v_mov_b32 v46, 0\n" \
  "v_mov_b32 v47, 0\n" \
  "s_nop 1\n" \
  "s_setprio 3\n" \
  "v_exp_f32 v16, v16\n" \
  "v_exp_f32 v17, v17\n" \
  "v_smfmac_f32_32x32x32_bf16 v[0:15], v[64:67], v[40:47], v72\n" \
  "v_exp_f32 v18, v18\n" \
  "v_exp_f32 v19, v19\n" \
  "v_exp_f32 v20, v20\n" \
  "v_exp_f32 v21, v21\n" \
  "s_waitcnt lgkmcnt(0)\n" \
  "v_mfma_f32_32x32x16_bf16 v[32:47], v[48:51], v[68:71], 0\n" \
  "ds_read_b128 v[48:51], v73 offset:2048\n" \
  "s_mov_b64 exec, %[vm]\n" \
  "ds_read_b128 v[60:63], v74 offset:1024\n" \
  "ds_read_b128 v[64:67], v74 offset:1536\n" \
  "s_mov_b64 exec, -1\n" \
  "v_exp_f32 v22, v22\n" \
  "v_exp_f32 v23, v23\n" \
  "v_exp_f32 v24, v24\n" \
  "v_exp_f32 v25, v25\n" \
  "v_smfmac_f32_32x32x32_bf16 v[0:15], v[52:55], v[16:23], v72\n" \
  "v_exp_f32 v26, v26\n" \
  "v_exp_f32 v27, v27\n" \
  "v_exp_f32 v28, v28\n" \
  "v_exp_f32 v29, v29\n" \
  "v_exp_f32 v30, v30\n" \
  "v_exp_f32 v31, v31\n" \
  "v_exp_f32 v32, v32\n" \
  "v_exp_f32 v33, v33\n" \
  "v_smfmac_f32_32x32x32_bf16 v[0:15], v[56:59], v[24:31], v72\n" \
  "v_exp_f32 v34, v34\n" \
  "v_exp_f32 v35, v35\n" \
  "v_exp_f32 v36, v36\n" \
  "v_exp_f32 v37, v37\n" \
  "s_waitcnt lgkmcnt(0)\n" \
  "v_mfma_f32_32x32x16_bf16 v[16:31], v[48:51], v[68:71], 0\n" \
  "ds_read_b128 v[48:51], v73 offset:3072\n" \
  "s_mov_b64 exec, %[vm]\n" \
  "ds_read_b128 v[52:55], v74 offset:2048\n" \
  "ds_read_b128 v[56:59], v74 offset:2560\n" \
  "s_mov_b64 exec, -1\n" \
  "buffer_load_dwordx2 v[76:77], v99, %[ws], 0 offen sc1\n" \
  "v_exp_f32 v38, v38\n" \
  "v_exp_f32 v39, v39\n" \
  "v_exp_f32 v40, v40\n" \
  "v_exp_f32 v41, v41\n" \
  "v_smfmac_f32_32x32x32_bf16 v[0:15], v[60:63], v[32:39], v72\n" \
  "v_exp_f32 v42, v42\n" \
  "v_exp_f32 v43, v43\n" \
  "v_exp_f32 v44, v44\n" \
  "v_exp_f32 v45, v45\n" \
  "v_exp_f32 v46, v46\n" \
  "v_exp_f32 v47, v47\n" \
  "s_setprio 2\n" \
  "v_exp_f32 v16, v16\n" \
  "v_exp_f32 v17, v17\n" \
  "v_smfmac_f32_32x32x32_bf16 v[0:15], v[64:67], v[40:47], v72\n" \
  "v_exp_f32 v18, v18\n" \
  "v_exp_f32 v19, v19\n" \
  "v_exp_f32 v20, v20\n" \
  "v_exp_f32 v21, v21\n" \
  "s_waitcnt lgkmcnt(0)\n" \
  "v_mfma_f32_32x32x16_bf16 v[32:47], v[48:51], v[68:71], 0\n" \
  "ds_read_b128 v[48:51], v73 offset:4096\n" \
  "s_mov_b64 exec, %[vm]\n" \
  "ds_read_b128 v[60:63], v74 offset:3072\n" \
  "ds_read_b128 v[64:67], v74 offset:3584\n" \
  "s_mov_b64 exec, -1\n" \
  "v_exp_f32 v22, v22\n" \
  "v_exp_f32 v23, v23\n" \
  "v_exp_f32 v24, v24\n" \
  "v_exp_f32 v25, v25\n" \
  "v_smfmac_f32_32x32x32_bf16 v[0:15], v[52:55], v[16:23], v72\n" \
  "v_exp_f32 v26, v26\n" \
  "v_exp_f32 v27, v27\n" \
  "v_exp_f32 v28, v28\n" \
  "v_exp_f32 v29, v29\n" \
  "v_exp_f32 v30, v30\n" \
  "v_exp_f32 v31, v31\n" \
  "v_exp_f32 v32, v32\n" \
  "v_exp_f32 v33, v33\n" \
  "v_smfmac_f32_32x32x32_bf16 v[0:15], v[56:59], v[24:31], v72\n" \
  "v_exp_f32 v34, v34\n" \
  "v_exp_f32 v35, v35\n" \
  "v_exp_f32 v36, v36\n" \
  "v_exp_f32 v37, v37\n" \
  "s_waitcnt lgkmcnt(0)\n" \
  "v_mfma_f32_32x32x16_bf16 v[16:31], v[48:51], v[68:71], 0\n" \
  "ds_read_b128 v[48:51], v73 offset:5120\n" \
  "s_mov_b64 exec, %[vm]\n" \
  "ds_read_b128 v[52:55], v74 offset:4096\n" \
  "ds_read_b128 v[56:59], v74 offset:4608\n" \
  "s_mov_b64 exec, -1\n" \
  "v_exp_f32 v38, v38\n" \
  "v_exp_f32 v39, v39\n" \
  "v_exp_f32 v40, v40\n" \
  "v_exp_f32 v41, v41\n" \
  "v_smfmac_f32_32x32x32_bf16 v[0:15], v[60:63], v[32:39], v72\n" \
  "v_exp_f32 v42, v42\n" \
  "v_exp_f32 v43, v43\n" \
  "v_exp_f32 v44, v44\n" \
  "v_exp_f32 v45, v45\n" \
  "v_exp_f32 v46, v46\n" \
  "v_exp_f32 v47, v47\n" \
  "s_setprio 1\n" \
  "v_exp_f32 v16, v16\n" \
  "v_exp_f32 v17, v17\n" \
  "v_smfmac_f32_32x32x32_bf16 v[0:15], v[64:67], v[40:47], v72\n" \
  "v_exp_f32 v18, v18\n" \
  "v_exp_f32 v19, v19\n" \
  "v_exp_f32 v20, v20\n" \
  "v_exp_f32 v21, v21\n" \
  "s_waitcnt lgkmcnt(0)\n" \
  "v_mfma_f32_32x32x16_bf16 v[32:47], v[48:51], v[68:71], 0\n" \
  "ds_read_b128 v[48:51], v73 offset:6144\n" \
  "s_mov_b64 exec, %[vm]\n" \
  "ds_read_b128 v[60:63], v74 offset:5120\n" \
  "ds_read_b128 v[64:67], v74 offset:5632\n" \
  "s_mov_b64 exec, -1\n" \
  "s_waitcnt vmcnt(0)\n" \
  "v_cmp_ne_u32 vcc, %[elo], v76\n" \
  "v_cmp_ne_u32 %[sp], %[ehi], v77\n" \
  "s_nop 1\n" \
  "s_or_b64 vcc, vcc, %[sp]\n" \
  "s_cbranch_vccnz 91f\n" \
  "90:\n" \
  "v_exp_f32 v22, v22\n" \
  "v_exp_f32 v23, v23\n" \
  "v_exp_f32 v24, v24\n" \
  "v_exp_f32 v25, v25\n" \
  "v_smfmac_f32_32x32x32_bf16 v[0:15], v[52:55], v[16:23], v72\n" \
  "v_exp_f32 v26, v26\n" \
  "v_exp_f32 v27, v27\n" \
  "v_exp_f32 v28, v28\n" \
  "v_exp_f32 v29, v29\n" \
  "v_exp_f32 v30, v30\n" \
  "v_exp_f32 v31, v31\n" \
  "v_exp_f32 v32, v32\n" \
  "v_exp_f32 v33, v33\n" \
  "v_smfmac_f32_32x32x32_bf16 v[0:15], v[56:59], v[24:31], v72\n" \
  "v_exp_f32 v34, v34\n" \
  "v_exp_f32 v35, v35\n" \
  "v_exp_f32 v36, v36\n" \
  "v_exp_f32 v37, v37\n" \
  "s_waitcnt lgkmcnt(0)\n" \
  "v_mfma_f32_32x32x16_bf16 v[16:31], v[48:51], v[68:71], 0\n" \
  "ds_read_b128 v[48:51], v73 offset:7168\n" \
  "s_mov_b64 exec, %[vm]\n" \
  "ds_read_b128 v[52:55], v74 offset:6144\n" \
  "ds_read_b128 v[56:59], v74 offset:6656\n" \
  "s_mov_b64 exec, -1\n" \
  "buffer_load_dwordx4 v[108:111], v94, %[ws], %[k1] offen sc1\n" \
  "buffer_load_dwordx4 v[112:115], v94, %[wv2], %[k1] offen sc1\n" \
  "v_exp_f32 v38, v38\n" \
  "v_exp_f32 v39, v39\n" \
  "v_exp_f32 v40, v40\n" \
  "v_exp_f32 v41, v41\n" \
  "v_smfmac_f32_32x32x32_bf16 v[0:15], v[60:63], v[32:39], v72\n" \
  "v_exp_f32 v42, v42\n" \
  "v_exp_f32 v43, v43\n" \
  "v_exp_f32 v44, v44\n" \
  "v_exp_f32 v45, v45\n" \
  "v_exp_f32 v46, v46\n" \
  "v_exp_f32 v47, v47\n" \
  "s_setprio 0\n" \
  "v_exp_f32 v16, v16\n" \
  "v_exp_f32 v17, v17\n" \
  "v_smfmac_f32_32x32x32_bf16 v[0:15], v[64:67], v[40:47], v72\n" \
  "v_exp_f32 v18, v18\n" \
  "v_exp_f32 v19, v19\n" \
  "v_exp_f32 v20, v20\n" \
  "v_exp_f32 v21, v21\n" \
  "s_waitcnt lgkmcnt(0)\n" \
  "v_mfma_f32_32x32x16_bf16 v[32:47], v[48:51], v[68:71], 0\n" \
  "s_mov_b64 exec, %[vm]\n" \
  "ds_read_b128 v[60:63], v74 offset:7168\n" \
  "ds_read_b128 v[64:67], v74 offset:7680\n" \
  "s_mov_b64 exec, -1\n" \
  "buffer_load_dwordx4 v[100:103], v94, %[ws], %[k2] offen sc1\n" \
  "buffer_load_dwordx4 v[104:107], v94, %[wv2], %[k2] offen sc1\n" \
  "v_exp_f32 v22, v22\n" \
  "v_exp_f32 v23, v23\n" \
  "v_exp_f32 v24, v24\n" \
  "v_exp_f32 v25, v25\n" \
  "v_smfmac_f32_32x32x32_bf16 v[0:15], v[52:55], v[16:23], v72\n" \
  "v_exp_f32 v26, v26\n" \
  "v_exp_f32 v27, v27\n" \
  "v_exp_f32 v28, v28\n" \
  "v_exp_f32 v29, v29\n" \
  "v_exp_f32 v30, v30\n" \
  "v_exp_f32 v31, v31\n" \
  "v_exp_f32 v32, v32\n" \
  "v_exp_f32 v33, v33\n" \
  "v_smfmac_f32_32x32x32_bf16 v[0:15], v[56:59], v[24:31], v72\n" \
  "v_exp_f32 v34, v34\n" \
  "v_exp_f32 v35, v35\n" \
  "v_exp_f32 v36, v36\n" \
  "v_exp_f32 v37, v37\n" \
  "s_waitcnt lgkmcnt(0)\n" \
  "v_exp_f32 v38, v38\n" \
  "v_exp_f32 v39, v39\n" \
  "v_exp_f32 v40, v40\n" \
  "v_exp_f32 v41, v41\n" \
  "v_smfmac_f32_32x32x32_bf16 v[0:15], v[60:63], v[32:39], v72\n" \
  "v_exp_f32 v42, v42\n" \
  "v_exp_f32 v43, v43\n" \
  "v_exp_f32 v44, v44\n" \
  "v_exp_f32 v45, v45\n" \
  "v_exp_f32 v46, v46\n" \
  "v_exp_f32 v47, v47\n" \
  "s_nop 1\n" \
  "v_smfmac_f32_32x32x32_bf16 v[0:15], v[64:67], v[40:47], v72\n" \
  "s_nop 7\n" \
  "s_nop 7\n" \
  "s_setprio 0\n" \
  "s_waitcnt vmcnt(2)\n" \
  "v_max3_u32 v92, v108, v109, v110\n" \
  "v_max3_u32 v92, v92, v111, v112\n" \
  "v_max3_u32 v92, v92, v113, v114\n" \
  "v_max_u32 v92, v92, v115\n" \
  "v_cmp_eq_u32 vcc, -1, v92\n" \
  "s_nop 1\n" \
  "s_cbranch_vccnz 1001f\n" \
  "1:\n" \
  "ds_write_b128 v95, v[108:111] offset:8192\n" \
  "ds_write_b128 v96, v[112:115] offset:8192\n" \
  "s_branch 2f\n" \
  "91:\n" \
  "s_mov_b32 %[sc], 0\n" \
  "92:\n" \
  "buffer_load_dwordx2 v[76:77], v99, %[ws], 0 offen sc1\n" \
  "s_sleep 1\n" \
  "s_add_u32 %[sc], %[sc], 1\n" \
  "s_waitcnt vmcnt(0)\n" \
  "v_cmp_ne_u32 vcc, %[elo], v76\n" \
  "v_cmp_ne_u32 %[sp], %[ehi], v77\n" \
  "s_nop 1\n" \
  "s_or_b64 vcc, vcc, %[sp]\n" \
  "s_cmp_lt_u32 %[sc], 0x20000\n" \
  "s_cselect_b64 %[sp], -1, 0\n" \
  "s_and_b64 vcc, vcc, %[sp]\n" \
  "s_cbranch_vccnz 92b\n" \
  "s_branch 90b\n" \
  "1001:\n" \
  "s_mov_b32 %[sc], 0\n" \
  "3001:\n" \
  "buffer_load_dwordx4 v[108:111], v94, %[ws], %[k1] offen sc1\n" \
  "buffer_load_dwordx4 v[112:115], v94, %[wv2], %[k1] offen sc1\n" \
  "s_sleep 1\n" \
  "s_add_u32 %[sc], %[sc], 1\n" \
  "s_waitcnt vmcnt(0)\n" \
  "v_max3_u32 v92, v108, v109, v110\n" \
  "v_max3_u32 v92, v92, v111, v112\n" \
  "v_max3_u32 v92, v92, v113, v114\n" \
  "v_max_u32 v92, v92, v115\n" \
  "v_cmp_eq_u32 vcc, -1, v92\n" \
  "s_cmp_lt_u32 %[sc], 0x20000\n" \
  "s_cselect_b64 %[sp], -1, 0\n" \
  "s_and_b64 vcc, vcc, %[sp]\n" \
  "s_cbranch_vccnz 3001b\n" \
  "s_branch 1b\n" \
  "2:\n"
#define ATTN_A_DBG \
  "ds_read_b128 v[48:51], v73 offset:0\n" \
  "v_mov_b32 v52, v91\n" \
  "v_mov_b32 v53, v91\n" \
  "v_mov_b32 v54, v91\n" \
  "v_mov_b32 v55, v91\n" \
  "v_mov_b32 v56, v91\n" \
  "v_mov_b32 v57, v91\n" \
  "v_mov_b32 v58, v91\n" \
  "v_mov_b32 v59, v91\n" \
  "v_mov_b32 v60, v91\n" \
  "v_mov_b32 v61, v91\n" \
  "v_mov_b32 v62, v91\n" \
  "v_mov_b32 v63, v91\n" \
  "v_mov_b32 v64, v91\n" \
  "v_mov_b32 v65, v91\n" \
  "v_mov_b32 v66, v91\n" \
  "v_mov_b32 v67, v91\n" \
  "s_mov_b64 exec, %[vm]\n" \
  "ds_read_b128 v[52:55], v74 offset:0\n" \
  "ds_read_b128 v[56:59], v74 offset:512\n" \
  "s_mov_b64 exec, -1\n" \
  "v_mov_b32 v0, 0\n" \
  "v_mov_b32 v1, 0\n" \
  "v_mov_b32 v2, 0\n" \
  "v_mov_b32 v3, 0\n" \
  "v_mov_b32 v4, 0\n" \
  "v_mov_b32 v5, 0\n" \
  "v_mov_b32 v6, 0\n" \
  "v_mov_b32 v7, 0\n" \
  "v_mov_b32 v8, 0\n" \
  "v_mov_b32 v9, 0\n" \
  "v_mov_b32 v10, 0\n" \
  "v_mov_b32 v11, 0\n" \
  "v_mov_b32 v12, 0\n" \
  "v_mov_b32 v13, 0\n" \
  "v_mov_b32 v14, 0\n" \
  "v_mov_b32 v15, 0\n" \
  "s_waitcnt lgkmcnt(2)\n" \
  "v_mfma_f32_32x32x16_bf16 v[16:31], v[48:51], v[68:71], 0\n" \
  "ds_read_b128 v[48:51], v73 offset:1024\n" \
  "v_mov_b32 v40, 0\n" \
  "v_mov_b32 v41, 0\n" \
  "v_mov_b32 v42, 0\n" \
  "v_mov_b32 v43, 0\n" \
  "v_mov_b32 v44, 0\n" \
  "v_mov_b32 v45, 0\n" \
  "v_mov_b32 v46, 0\n" \
  "v_mov_b32 v47, 0\n" \
  "s_nop 1\n" \
  "s_setprio 3\n" \
  "v_exp_f32 v16, v16\n" \
  "v_exp_f32 v17, v17\n" \
  "v_smfmac_f32_32x32x32_bf16 v[0:15], v[64:67], v[40:47], v72\n" \
  "v_exp_f32 v18, v18\n" \
  "v_exp_f32 v19, v19\n" \
  "v_exp_f32 v20, v20\n" \
  "v_exp_f32 v21, v21\n" \
  "s_waitcnt lgkmcnt(0)\n" \
  "v_mfma_f32_32x32x16_bf16 v[32:47], v[48:51], v[68:71], 0\n" \
  "ds_read_b128 v[48:51], v73 offset:2048\n" \
  "s_mov_b64 exec, %[vm]\n" \
  "ds_read_b128 v[60:63], v74 offset:1024\n" \
  "ds_read_b128 v[64:67], v74 offset:1536\n" \
  "s_mov_b64 exec, -1\n" \
  "v_exp_f32 v22, v22\n" \
  "v_exp_f32 v23, v23\n" \
  "v_exp_f32 v24, v24\n" \
  "v_exp_f32 v25, v25\n" \
  "v_smfmac_f32_32x32x32_bf16 v[0:15], v[52:55], v[16:23], v72\n" \
  "v_exp_f32 v26, v26\n" \
  "v_exp_f32 v27, v27\n" \
  "v_exp_f32 v28, v28\n" \
  "v_exp_f32 v29, v29\n" \
  "v_exp_f32 v30, v30\n" \
  "v_exp_f32 v31, v31\n" \
  "v_exp_f32 v32, v32\n" \
  "v_exp_f32 v33, v33\n" \
  "v_smfmac_f32_32x32x32_bf16 v[0:15], v[56:59], v[24:31], v72\n" \
  "v_exp_f32 v34, v34\n" \
  "v_exp_f32 v35, v35\n" \
  "v_exp_f32 v36, v36\n" \
  "v_exp_f32 v37, v37\n" \
  "s_waitcnt lgkmcnt(0)\n" \
  "v_mfma_f32_32x32x16_bf16 v[16:31], v[48:51], v[68:71], 0\n" \
  "ds_read_b128 v[48:51], v73 offset:3072\n" \
  "s_mov_b64 exec, %[vm]\n" \
  "ds_read_b128 v[52:55], v74 offset:2048\n" \
  "ds_read_b128 v[56:59], v74 offset:2560\n" \
  "s_mov_b64 exec, -1\n" \
  "buffer_load_dwordx2 v[76:77], v99, %[ws], 0 offen sc1\n" \
  "v_exp_f32 v38, v38\n" \
  "v_exp_f32 v39, v39\n" \
  "v_exp_f32 v40, v40\n" \
  "v_exp_f32 v41, v41\n" \
  "v_smfmac_f32_32x32x32_bf16 v[0:15], v[60:63], v[32:39], v72\n" \
  "v_exp_f32 v42, v42\n" \
  "v_exp_f32 v43, v43\n" \
  "v_exp_f32 v44, v44\n" \
  "v_exp_f32 v45, v45\n" \
  "v_exp_f32 v46, v46\n" \
  "v_exp_f32 v47, v47\n" \
  "s_setprio 2\n" \
  "v_exp_f32 v16, v16\n" \
  "v_exp_f32 v17, v17\n" \
  "v_smfmac_f32_32x32x32_bf16 v[0:15], v[64:67], v[40:47], v72\n" \
  "v_exp_f32 v18, v18\n" \
  "v_exp_f32 v19, v19\n" \
  "v_exp_f32 v20, v20\n" \
  "v_exp_f32 v21, v21\n" \
  "s_waitcnt lgkmcnt(0)\n" \
  "v_mfma_f32_32x32x16_bf16 v[32:47], v[48:51], v[68:71], 0\n" \
  "ds_read_b128 v[48:51], v73 offset:4096\n" \
  "s_mov_b64 exec, %[vm]\n" \
  "ds_read_b128 v[60:63], v74 offset:3072\n" \
  "ds_read_b128 v[64:67], v74 offset:3584\n" \
  "s_mov_b64 exec, -1\n" \
  "v_exp_f32 v22, v22\n" \
  "v_exp_f32 v23, v23\n" \
  "v_exp_f32 v24, v24\n" \
  "v_exp_f32 v25, v25\n" \
  "v_smfmac_f32_32x32x32_bf16 v[0:15], v[52:55], v[16:23], v72\n" \
  "v_exp_f32 v26, v26\n" \
  "v_exp_f32 v27, v27\n" \
  "v_exp_f32 v28, v28\n" \
  "v_exp_f32 v29, v29\n" \
  "v_exp_f32 v30, v30\n" \
  "v_exp_f32 v31, v31\n" \
  "v_exp_f32 v32, v32\n" \
  "v_exp_f32 v33, v33\n" \
  "v_smfmac_f32_32x32x32_bf16 v[0:15], v[56:59], v[24:31], v72\n" \
  "v_exp_f32 v34, v34\n" \
  "v_exp_f32 v35, v35\n" \
  "v_exp_f32 v36, v36\n" \
  "v_exp_f32 v37, v37\n" \
  "s_waitcnt lgkmcnt(0)\n" \
  "v_mfma_f32_32x32x16_bf16 v[16:31], v[48:51], v[68:71], 0\n" \
  "ds_read_b128 v[48:51], v73 offset:5120\n" \
  "s_mov_b64 exec, %[vm]\n" \
  "ds_read_b128 v[52:55], v74 offset:4096\n" \
  "ds_read_b128 v[56:59], v74 offset:4608\n" \
  "s_mov_b64 exec, -1\n" \
  "v_exp_f32 v38, v38\n" \
  "v_exp_f32 v39, v39\n" \
  "v_exp_f32 v40, v40\n" \
  "v_exp_f32 v41, v41\n" \
  "v_smfmac_f32_32x32x32_bf16 v[0:15], v[60:63], v[32:39], v72\n" \
  "v_exp_f32 v42, v42\n" \
  "v_exp_f32 v43, v43\n" \
  "v_exp_f32 v44, v44\n" \
  "v_exp_f32 v45, v45\n" \
  "v_exp_f32 v46, v46\n" \
  "v_exp_f32 v47, v47\n" \
  "s_setprio 1\n" \
  "v_exp_f32 v16, v16\n" \
  "v_exp_f32 v17, v17\n" \
  "v_smfmac_f32_32x32x32_bf16 v[0:15], v[64:67], v[40:47], v72\n" \
  "v_exp_f32 v18, v18\n" \
  "v_exp_f32 v19, v19\n" \
  "v_exp_f32 v20, v20\n" \
  "v_exp_f32 v21, v21\n" \
  "s_waitcnt lgkmcnt(0)\n" \
  "v_mfma_f32_32x32x16_bf16 v[32:47], v[48:51], v[68:71], 0\n" \
  "ds_read_b128 v[48:51], v73 offset:6144\n" \
  "s_mov_b64 exec, %[vm]\n" \
  "ds_read_b128 v[60:63], v74 offset:5120\n" \
  "ds_read_b128 v[64:67], v74 offset:5632\n" \
  "s_mov_b64 exec, -1\n" \
  "s_memrealtime %[d0]\n" \
  "s_waitcnt vmcnt(0)\n" \
  "v_cmp_ne_u32 vcc, %[elo], v76\n" \
  "v_cmp_ne_u32 %[sp], %[ehi], v77\n" \
  "s_nop 1\n" \
  "s_or_b64 vcc, vcc, %[sp]\n" \
  "s_cbranch_vccnz 91f\n" \
  "90:\n" \
  "s_memrealtime %[d1]\n" \
  "v_exp_f32 v22, v22\n" \
  "v_exp_f32 v23, v23\n" \
  "v_exp_f32 v24, v24\n" \
  "v_exp_f32 v25, v25\n" \
  "v_smfmac_f32_32x32x32_bf16 v[0:15], v[52:55], v[16:23], v72\n" \
  "v_exp_f32 v26, v26\n" \
  "v_exp_f32 v27, v27\n" \
  "v_exp_f32 v28, v28\n" \
  "v_exp_f32 v29, v29\n" \
  "v_exp_f32 v30, v30\n" \
  "v_exp_f32 v31, v31\n" \
  "v_exp_f32 v32, v32\n" \
  "v_exp_f32 v33, v33\n" \
  "v_smfmac_f32_32x32x32_bf16 v[0:15], v[56:59], v[24:31], v72\n" \
  "v_exp_f32 v34, v34\n" \
  "v_exp_f32 v35, v35\n" \
  "v_exp_f32 v36, v36\n" \
  "v_exp_f32 v37, v37\n" \
  "s_waitcnt lgkmcnt(0)\n" \
  "v_mfma_f32_32x32x16_bf16 v[16:31], v[48:51], v[68:71], 0\n" \
  "ds_read_b128 v[48:51], v73 offset:7168\n" \
  "s_mov_b64 exec, %[vm]\n" \
  "ds_read_b128 v[52:55], v74 offset:6144\n" \
  "ds_read_b128 v[56:59], v74 offset:6656\n" \
  "s_mov_b64 exec, -1\n" \
  "buffer_load_dwordx4 v[108:111], v94, %[ws], %[k1] offen sc1\n" \
  "buffer_load_dwordx4 v[112:115], v94, %[wv2], %[k1] offen sc1\n" \
  "v_exp_f32 v38, v38\n" \
  "v_exp_f32 v39, v39\n" \
  "v_exp_f32 v40, v40\n" \
  "v_exp_f32 v41, v41\n" \
  "v_smfmac_f32_32x32x32_bf16 v[0:15], v[60:63], v[32:39], v72\n" \
  "v_exp_f32 v42, v42\n" \
  "v_exp_f32 v43, v43\n" \
  "v_exp_f32 v44, v44\n" \
  "v_exp_f32 v45, v45\n" \
  "v_exp_f32 v46, v46\n" \
  "v_exp_f32 v47, v47\n" \
  "s_setprio 0\n" \
  "v_exp_f32 v16, v16\n" \
  "v_exp_f32 v17, v17\n" \
  "v_smfmac_f32_32x32x32_bf16 v[0:15], v[64:67], v[40:47], v72\n" \
  "v_exp_f32 v18, v18\n" \
  "v_exp_f32 v19, v19\n" \
  "v_exp_f32 v20, v20\n" \
  "v_exp_f32 v21, v21\n" \
  "s_waitcnt lgkmcnt(0)\n" \
  "v_mfma_f32_32x32x16_bf16 v[32:47], v[48:51], v[68:71], 0\n" \
  "s_mov_b64 exec, %[vm]\n" \
  "ds_read_b128 v[60:63], v74 offset:7168\n" \
  "ds_read_b128 v[64:67], v74 offset:7680\n" \
  "s_mov_b64 exec, -1\n" \
  "buffer_load_dwordx4 v[100:103], v94, %[ws], %[k2] offen sc1\n" \
  "buffer_load_dwordx4 v[104:107], v94, %[wv2], %[k2] offen sc1\n" \
  "v_exp_f32 v22, v22\n" \
  "v_exp_f32 v23, v23\n" \
  "v_exp_f32 v24, v24\n" \
  "v_exp_f32 v25, v25\n" \
  "v_smfmac_f32_32x32x32_bf16 v[0:15], v[52:55], v[16:23], v72\n" \
  "v_exp_f32 v26, v26\n" \
  "v_exp_f32 v27, v27\n" \
  "v_exp_f32 v28, v28\n" \
  "v_exp_f32 v29, v29\n" \
  "v_exp_f32 v30, v30\n" \
  "v_exp_f32 v31, v31\n" \
  "v_exp_f32 v32, v32\n" \
  "v_exp_f32 v33, v33\n" \
  "v_smfmac_f32_32x32x32_bf16 v[0:15], v[56:59], v[24:31], v72\n" \
  "v_exp_f32 v34, v34\n" \
  "v_exp_f32 v35, v35\n" \
  "v_exp_f32 v36, v36\n" \
  "v_exp_f32 v37, v37\n" \
  "s_waitcnt lgkmcnt(0)\n" \
  "v_exp_f32 v38, v38\n" \
  "v_exp_f32 v39, v39\n" \
  "v_exp_f32 v40, v40\n" \
  "v_exp_f32 v41, v41\n" \
  "v_smfmac_f32_32x32x32_bf16 v[0:15], v[60:63], v[32:39], v72\n" \
  "v_exp_f32 v42, v42\n" \
  "v_exp_f32 v43, v43\n" \
  "v_exp_f32 v44, v44\n" \
  "v_exp_f32 v45, v45\n" \
  "v_exp_f32 v46, v46\n" \
  "v_exp_f32 v47, v47\n" \
  "s_nop 1\n" \
  "v_smfmac_f32_32x32x32_bf16 v[0:15], v[64:67], v[40:47], v72\n" \
  "s_nop 7\n" \
  "s_nop 7\n" \
  "s_setprio 0\n" \
  "s_waitcnt vmcnt(2)\n" \
  "s_memrealtime %[d2]\n" \
  "v_max3_u32 v92, v108, v109, v110\n" \
  "v_max3_u32 v92, v92, v111, v112\n" \
  "v_max3_u32 v92, v92, v113, v114\n" \
  "v_max_u32 v92, v92, v115\n" \
  "v_cmp_eq_u32 vcc, -1, v92\n" \
  "s_nop 1\n" \
  "s_cbranch_vccnz 1051f\n" \
  "51:\n" \
  "s_memrealtime %[d3]\n" \
  "s_waitcnt lgkmcnt(0)\n" \
  "ds_write_b128 v95, v[108:111] offset:8192\n" \
  "ds_write_b128 v96, v[112:115] offset:8192\n" \
  "s_branch 52f\n" \
  "91:\n" \
  "s_mov_b32 %[sc], 0\n" \
  "92:\n" \
  "buffer_load_dwordx2 v[76:77], v99, %[ws], 0 offen sc1\n" \
  "s_sleep 1\n" \
  "s_add_u32 %[sc], %[sc], 1\n" \
  "s_waitcnt vmcnt(0)\n" \
  "v_cmp_ne_u32 vcc, %[elo], v76\n" \
  "v_cmp_ne_u32 %[sp], %[ehi], v77\n" \
  "s_nop 1\n" \
  "s_or_b64 vcc, vcc, %[sp]\n" \
  "s_cmp_lt_u32 %[sc], 0x20000\n" \
  "s_cselect_b64 %[sp], -1, 0\n" \
  "s_and_b64 vcc, vcc, %[sp]\n" \
  "s_cbranch_vccnz 92b\n" \
  "s_branch 90b\n" \
  "1051:\n" \
  "s_mov_b32 %[sc], 0\n" \
  "3051:\n" \
  "buffer_load_dwordx4 v[108:111], v94, %[ws], %[k1] offen sc1\n" \
  "buffer_load_dwordx4 v[112:115], v94, %[wv2], %[k1] offen sc1\n" \
  "s_sleep 1\n" \
  "s_add_u32 %[sc], %[sc], 1\n" \
  "s_waitcnt vmcnt(0)\n" \
  "v_max3_u32 v92, v108, v109, v110\n" \
  "v_max3_u32 v92, v92, v111, v112\n" \
  "v_max3_u32 v92, v92, v113, v114\n" \
  "v_max_u32 v92, v92, v115\n" \
  "v_cmp_eq_u32 vcc, -1, v92\n" \
  "s_cmp_lt_u32 %[sc], 0x20000\n" \
  "s_cselect_b64 %[sp], -1, 0\n" \
  "s_and_b64 vcc, vcc, %[sp]\n" \
  "s_cbranch_vccnz 3051b\n" \
  "s_branch 51b\n" \
  "52:\n"
#define ATTN_B \
  "ds_read_b128 v[48:51], v73 offset:8192\n" \
  "v_mov_b32 v52, v91\n" \
  "v_mov_b32 v53, v91\n" \
  "v_mov_b32 v54, v91\n" \
  "v_mov_b32 v55, v91\n" \
  "v_mov_b32 v56, v91\n" \
  "v_mov_b32 v57, v91\n" \
  "v_mov_b32 v58, v91\n" \
  "v_mov_b32 v59, v91\n" \
  "v_mov_b32 v60, v91\n" \
  "v_mov_b32 v61, v91\n" \
  "v_mov_b32 v62, v91\n" \
  "v_mov_b32 v63, v91\n" \
  "v_mov_b32 v64, v91\n" \
  "v_mov_b32 v65, v91\n" \
  "v_mov_b32 v66, v91\n" \
  "v_mov_b32 v67, v91\n" \
  "s_mov_b64 exec, %[vm]\n" \
  "ds_read_b128 v[52:55], v74 offset:8192\n" \
  "ds_read_b128 v[56:59], v74 offset:8704\n" \
  "s_mov_b64 exec, -1\n" \
  "s_waitcnt lgkmcnt(2)\n" \
  "v_mfma_f32_32x32x16_bf16 v[16:31], v[48:51], v[68:71], 0\n" \
  "ds_read_b128 v[48:51], v73 offset:9216\n" \
  "v_mov_b32 v40, 0\n" \
  "v_mov_b32 v41, 0\n" \
  "v_mov_b32 v42, 0\n" \
  "v_mov_b32 v43, 0\n" \
  "v_mov_b32 v44, 0\n" \
  "v_mov_b32 v45, 0\n" \
  "v_mov_b32 v46, 0\n" \
  "v_mov_b32 v47, 0\n" \
  "s_nop 1\n" \
  "s_setprio 3\n" \
  "v_exp_f32 v16, v16\n" \
  "v_exp_f32 v17, v17\n" \
  "v_smfmac_f32_32x32x32_bf16 v[0:15], v[64:67], v[40:47], v72\n" \
  "v_exp_f32 v18, v18\n" \
  "v_exp_f32 v19, v19\n" \
  "v_exp_f32 v20, v20\n" \
  "v_exp_f32 v21, v21\n" \
  "s_waitcnt lgkmcnt(0)\n" \
  "v_mfma_f32_32x32x16_bf16 v[32:47], v[48:51], v[68:71], 0\n" \
  "ds_read_b128 v[48:51], v73 offset:10240\n" \
  "s_mov_b64 exec, %[vm]\n" \
  "ds_read_b128 v[60:63], v74 offset:9216\n" \
  "ds_read_b128 v[64:67], v74 offset:9728\n" \
  "s_mov_b64 exec, -1\n" \
  "s_waitcnt vmcnt(1)\n" \
  "v_max3_u32 v92, v100, v101, v102\n" \
  "v_max3_u32 v92, v92, v103, v104\n" \
  "v_max3_u32 v92, v92, v105, v106\n" \
  "v_max_u32 v92, v92, v107\n" \
  "v_cmp_eq_u32 vcc, -1, v92\n" \
  "s_nop 1\n" \
  "s_cbranch_vccnz 1011f\n" \
  "11:\n" \
  "ds_write_b128 v95, v[100:103] offset:16384\n" \
  "ds_write_b128 v96, v[104:107] offset:16384\n" \
  "buffer_load_dwordx4 v[108:111], v94, %[ws], %[k3] offen sc1\n" \
  "buffer_load_dwordx4 v[112:115], v94, %[wv2], %[k3] offen sc1\n" \
  "v_exp_f32 v22, v22\n" \
  "v_exp_f32 v23, v23\n" \
  "v_exp_f32 v24, v24\n" \
  "v_exp_f32 v25, v25\n" \
  "v_smfmac_f32_32x32x32_bf16 v[0:15], v[52:55], v[16:23], v72\n" \
  "v_exp_f32 v26, v26\n" \
  "v_exp_f32 v27, v27\n" \
  "v_exp_f32 v28, v28\n" \
  "v_exp_f32 v29, v29\n" \
  "v_exp_f32 v30, v30\n" \
  "v_exp_f32 v31, v31\n" \
  "v_exp_f32 v32, v32\n" \
  "v_exp_f32 v33, v33\n" \
  "v_smfmac_f32_32x32x32_bf16 v[0:15], v[56:59], v[24:31], v72\n" \
  "v_exp_f32 v34, v34\n" \
  "v_exp_f32 v35, v35\n" \
  "v_exp_f32 v36, v36\n" \
  "v_exp_f32 v37, v37\n" \
  "s_waitcnt lgkmcnt(0)\n" \
  "v_mfma_f32_32x32x16_bf16 v[16:31], v[48:51], v[68:71], 0\n" \
  "ds_read_b128 v[48:51], v73 offset:11264\n" \
  "s_mov_b64 exec, %[vm]\n" \
  "ds_read_b128 v[52:55], v74 offset:10240\n" \
  "ds_read_b128 v[56:59], v74 offset:10752\n" \
  "s_mov_b64 exec, -1\n" \
  "s_mov_b64 exec, 1\n" \
  "ds_add_u32 v89, v90 offset:8\n" \
  "s_mov_b64 exec, -1\n" \
  "v_exp_f32 v38, v38\n" \
  "v_exp_f32 v39, v39\n" \
  "v_exp_f32 v40, v40\n" \
  "v_exp_f32 v41, v41\n" \
  "v_smfmac_f32_32x32x32_bf16 v[0:15], v[60:63], v[32:39], v72\n" \
  "v_exp_f32 v42, v42\n" \
  "v_exp_f32 v43, v43\n" \
  "v_exp_f32 v44, v44\n" \
  "v_exp_f32 v45, v45\n" \
  "v_exp_f32 v46, v46\n" \
  "v_exp_f32 v47, v47\n" \
  "v_exp_f32 v16, v16\n" \
  "v_exp_f32 v17, v17\n" \
  "v_smfmac_f32_32x32x32_bf16 v[0:15], v[64:67], v[40:47], v72\n" \
  "v_exp_f32 v18, v18\n" \
  "v_exp_f32 v19, v19\n" \
  "v_exp_f32 v20, v20\n" \
  "v_exp_f32 v21, v21\n" \
  "s_waitcnt lgkmcnt(0)\n" \
  "v_mfma_f32_32x32x16_bf16 v[32:47], v[48:51], v[68:71], 0\n" \
  "ds_read_b128 v[48:51], v73 offset:12288\n" \
  "s_mov_b64 exec, %[vm]\n" \
  "ds_read_b128 v[60:63], v74 offset:11264\n" \
  "ds_read_b128 v[64:67], v74 offset:11776\n" \
  "s_mov_b64 exec, -1\n" \
  "v_mov_b32 v84, -1\n" \
  "v_mov_b32 v85, -1\n" \
  "v_mov_b32 v86, -1\n" \
  "v_mov_b32 v87, -1\n" \
  "buffer_store_dwordx4 v[84:87], v94, %[ws], %[sof] offen sc1\n" \
  "v_exp_f32 v22, v22\n" \
  "v_exp_f32 v23, v23\n" \
  "v_exp_f32 v24, v24\n" \
  "v_exp_f32 v25, v25\n" \
  "v_smfmac_f32_32x32x32_bf16 v[0:15], v[52:55], v[16:23], v72\n" \
  "v_exp_f32 v26, v26\n" \
  "v_exp_f32 v27, v27\n" \
  "v_exp_f32 v28, v28\n" \
  "v_exp_f32 v29, v29\n" \
  "v_exp_f32 v30, v30\n" \
  "v_exp_f32 v31, v31\n" \
  "v_exp_f32 v32, v32\n" \
  "v_exp_f32 v33, v33\n" \
  "v_smfmac_f32_32x32x32_bf16 v[0:15], v[56:59], v[24:31], v72\n" \
  "v_exp_f32 v34, v34\n" \
  "v_exp_f32 v35, v35\n" \
  "v_exp_f32 v36, v36\n" \
  "v_exp_f32 v37, v37\n" \
  "s_waitcnt lgkmcnt(0)\n" \
  "v_mfma_f32_32x32x16_bf16 v[16:31], v[48:51], v[68:71], 0\n" \
  "ds_read_b128 v[48:51], v73 offset:13312\n" \
  "s_mov_b64 exec, %[vm]\n" \
  "ds_read_b128 v[52:55], v74 offset:12288\n" \
  "ds_read_b128 v[56:59], v74 offset:12800\n" \
  "s_mov_b64 exec, -1\n" \
  "buffer_load_dwordx4 v[192:195], v98, %[refa], %[sef] offen\n" \
  "s_add_u32 %[sef], %[sef], %[sefs]\n" \
  "v_exp_f32 v38, v38\n" \
  "v_exp_f32 v39, v39\n" \
  "v_exp_f32 v40, v40\n" \
  "v_exp_f32 v41, v41\n" \
  "v_smfmac_f32_32x32x32_bf16 v[0:15], v[60:63], v[32:39], v72\n" \
  "v_exp_f32 v42, v42\n" \
  "v_exp_f32 v43, v43\n" \
  "v_exp_f32 v44, v44\n" \
  "v_exp_f32 v45, v45\n" \
  "v_exp_f32 v46, v46\n" \
  "v_exp_f32 v47, v47\n" \
  "s_setprio 2\n" \
  "v_exp_f32 v16, v16\n" \
  "v_exp_f32 v17, v17\n" \
  "v_smfmac_f32_32x32x32_bf16 v[0:15], v[64:67], v[40:47], v72\n" \
  "v_exp_f32 v18, v18\n" \
  "v_exp_f32 v19, v19\n" \
  "v_exp_f32 v20, v20\n" \
  "v_exp_f32 v21, v21\n" \
  "s_waitcnt lgkmcnt(0)\n" \
  "v_mfma_f32_32x32x16_bf16 v[32:47], v[48:51], v[68:71], 0\n" \
  "ds_read_b128 v[48:51], v73 offset:14336\n" \
  "s_mov_b64 exec, %[vm]\n" \
  "ds_read_b128 v[60:63], v74 offset:13312\n" \
  "ds_read_b128 v[64:67], v74 offset:13824\n" \
  "s_mov_b64 exec, -1\n" \
  "buffer_store_dwordx4 v[84:87], v88, %[ws], %[smi] offen sc1\n" \
  "v_exp_f32 v22, v22\n" \
  "v_exp_f32 v23, v23\n" \
  "v_exp_f32 v24, v24\n" \
  "v_exp_f32 v25, v25\n" \
  "v_smfmac_f32_32x32x32_bf16 v[0:15], v[52:55], v[16:23], v72\n" \
  "v_exp_f32 v26, v26\n" \
  "v_exp_f32 v27, v27\n" \
  "v_exp_f32 v28, v28\n" \
  "v_exp_f32 v29, v29\n" \
  "v_exp_f32 v30, v30\n" \
  "v_exp_f32 v31, v31\n" \
  "v_exp_f32 v32, v32\n" \
  "v_exp_f32 v33, v33\n" \
  "v_smfmac_f32_32x32x32_bf16 v[0:15], v[56:59], v[24:31], v72\n" \
  "v_exp_f32 v34, v34\n" \
  "v_exp_f32 v35, v35\n" \
  "v_exp_f32 v36, v36\n" \
  "v_exp_f32 v37, v37\n" \
  "s_waitcnt lgkmcnt(0)\n" \
  "v_mfma_f32_32x32x16_bf16 v[16:31], v[48:51], v[68:71], 0\n" \
  "ds_read_b128 v[48:51], v73 offset:15360\n" \
  "s_mov_b64 exec, %[vm]\n" \
  "ds_read_b128 v[52:55], v74 offset:14336\n" \
  "ds_read_b128 v[56:59], v74 offset:14848\n" \
  "s_mov_b64 exec, -1\n" \
  "ds_read_b32 v93, v89 offset:8\n" \
  "v_exp_f32 v38, v38\n" \
  "v_exp_f32 v39, v39\n" \
  "v_exp_f32 v40, v40\n" \
  "v_exp_f32 v41, v41\n" \
  "v_smfmac_f32_32x32x32_bf16 v[0:15], v[60:63], v[32:39], v72\n" \
  "v_exp_f32 v42, v42\n" \
  "v_exp_f32 v43, v43\n" \
  "v_exp_f32 v44, v44\n" \
  "v_exp_f32 v45, v45\n" \
  "v_exp_f32 v46, v46\n" \
  "v_exp_f32 v47, v47\n" \
  "v_exp_f32 v16, v16\n" \
  "v_exp_f32 v17, v17\n" \
  "v_smfmac_f32_32x32x32_bf16 v[0:15], v[64:67], v[40:47], v72\n" \
  "v_exp_f32 v18, v18\n" \
  "v_exp_f32 v19, v19\n" \
  "v_exp_f32 v20, v20\n" \
  "v_exp_f32 v21, v21\n" \
  "s_waitcnt lgkmcnt(0)\n" \
  "v_mfma_f32_32x32x16_bf16 v[32:47], v[48:51], v[68:71], 0\n" \
  "v_readfirstlane_b32 %[sc], v93\n" \
  "s_nop 0\n" \
  "s_cmp_lt_u32 %[sc], 8\n" \
  "s_cbranch_scc1 1012f\n" \
  "12:\n" \
  "ds_read_b128 v[48:51], v73 offset:16384\n" \
  "s_mov_b64 exec, %[vm]\n" \
  "ds_read_b128 v[60:63], v74 offset:15360\n" \
  "ds_read_b128 v[64:67], v74 offset:15872\n" \
  "s_mov_b64 exec, -1\n" \
  "buffer_load_dwordx4 v[128:131], v97, %[rwo], %[swo] offen\n" \
  "s_add_u32 %[swo], %[swo], 16384\n" \
  "v_exp_f32 v22, v22\n" \
  "v_exp_f32 v23, v23\n" \
  "v_exp_f32 v24, v24\n" \
  "v_exp_f32 v25, v25\n" \
  "v_smfmac_f32_32x32x32_bf16 v[0:15], v[52:55], v[16:23], v72\n" \
  "v_exp_f32 v26, v26\n" \
  "v_exp_f32 v27, v27\n" \
  "v_exp_f32 v28, v28\n" \
  "v_exp_f32 v29, v29\n" \
  "v_exp_f32 v30, v30\n" \
  "v_exp_f32 v31, v31\n" \
  "v_exp_f32 v32, v32\n" \
  "v_exp_f32 v33, v33\n" \
  "v_smfmac_f32_32x32x32_bf16 v[0:15], v[56:59], v[24:31], v72\n" \
  "v_exp_f32 v34, v34\n" \
  "v_exp_f32 v35, v35\n" \
  "v_exp_f32 v36, v36\n" \
  "v_exp_f32 v37, v37\n" \
  "s_waitcnt lgkmcnt(0)\n" \
  "v_mfma_f32_32x32x16_bf16 v[16:31], v[48:51], v[68:71], 0\n" \
  "ds_read_b128 v[48:51], v73 offset:17408\n" \
  "s_mov_b64 exec, %[vm]\n" \
  "ds_read_b128 v[52:55], v74 offset:16384\n" \
  "ds_read_b128 v[56:59], v74 offset:16896\n" \
  "s_mov_b64 exec, -1\n" \
  "v_exp_f32 v38, v38\n" \
  "v_exp_f32 v39, v39\n" \
  "v_exp_f32 v40, v40\n" \
  "v_exp_f32 v41, v41\n" \
  "v_smfmac_f32_32x32x32_bf16 v[0:15], v[60:63], v[32:39], v72\n" \
  "v_exp_f32 v42, v42\n" \
  "v_exp_f32 v43, v43\n" \
  "v_exp_f32 v44, v44\n" \
  "v_exp_f32 v45, v45\n" \
  "v_exp_f32 v46, v46\n" \
  "v_exp_f32 v47, v47\n" \
  "s_setprio 1\n" \
  "v_exp_f32 v16, v16\n" \
  "v_exp_f32 v17, v17\n" \
  "v_smfmac_f32_32x32x32_bf16 v[0:15], v[64:67], v[40:47], v72\n" \
  "v_exp_f32 v18, v18\n" \
  "v_exp_f32 v19, v19\n" \
  "v_exp_f32 v20, v20\n" \
  "v_exp_f32 v21, v21\n" \
  "s_waitcnt lgkmcnt(0)\n" \
  "v_mfma_f32_32x32x16_bf16 v[32:47], v[48:51], v[68:71], 0\n" \
  "ds_read_b128 v[48:51], v73 offset:18432\n" \
  "s_mov_b64 exec, %[vm]\n" \
  "ds_read_b128 v[60:63], v74 offset:17408\n" \
  "ds_read_b128 v[64:67], v74 offset:17920\n" \
  "s_mov_b64 exec, -1\n" \
  "s_waitcnt vmcnt(4)\n" \
  "v_max3_u32 v92, v108, v109, v110\n" \
  "v_max3_u32 v92, v92, v111, v112\n" \
  "v_max3_u32 v92, v92, v113, v114\n" \
  "v_max_u32 v92, v92, v115\n" \
  "v_cmp_eq_u32 vcc, -1, v92\n" \
  "s_nop 1\n" \
  "s_cbranch_vccnz 1013f\n" \
  "13:\n" \
  "ds_write_b128 v95, v[108:111] offset:24576\n" \
  "ds_write_b128 v96, v[112:115] offset:24576\n" \
  "buffer_load_dwordx4 v[100:103], v94, %[ws], %[k4] offen sc1\n" \
  "buffer_load_dwordx4 v[104:107], v94, %[wv2], %[k4] offen sc1\n" \
  "v_exp_f32 v22, v22\n" \
  "v_exp_f32 v23, v23\n" \
  "v_exp_f32 v24, v24\n" \
  "v_exp_f32 v25, v25\n" \
  "v_smfmac_f32_32x32x32_bf16 v[0:15], v[52:55], v[16:23], v72\n" \
  "v_exp_f32 v26, v26\n" \
  "v_exp_f32 v27, v27\n" \
  "v_exp_f32 v28, v28\n" \
  "v_exp_f32 v29, v29\n" \
  "v_exp_f32 v30, v30\n" \
  "v_exp_f32 v31, v31\n" \
  "v_exp_f32 v32, v32\n" \
  "v_exp_f32 v33, v33\n" \
  "v_smfmac_f32_32x32x32_bf16 v[0:15], v[56:59], v[24:31], v72\n" \
  "v_exp_f32 v34, v34\n" \
  "v_exp_f32 v35, v35\n" \
  "v_exp_f32 v36, v36\n" \
  "v_exp_f32 v37, v37\n" \
  "s_waitcnt lgkmcnt(0)\n" \
  "v_mfma_f32_32x32x16_bf16 v[16:31], v[48:51], v[68:71], 0\n" \
  "ds_read_b128 v[48:51], v73 offset:19456\n" \
  "s_mov_b64 exec, %[vm]\n" \
  "ds_read_b128 v[52:55], v74 offset:18432\n" \
  "ds_read_b128 v[56:59], v74 offset:18944\n" \
  "s_mov_b64 exec, -1\n" \
  "buffer_load_dwordx4 v[132:135], v97, %[rwo], %[swo] offen\n" \
  "s_add_u32 %[swo], %[swo], 16384\n" \
  "v_exp_f32 v38, v38\n" \
  "v_exp_f32 v39, v39\n" \
  "v_exp_f32 v40, v40\n" \
  "v_exp_f32 v41, v41\n" \
  "v_smfmac_f32_32x32x32_bf16 v[0:15], v[60:63], v[32:39], v72\n" \
  "v_exp_f32 v42, v42\n" \
  "v_exp_f32 v43, v43\n" \
  "v_exp_f32 v44, v44\n" \
  "v_exp_f32 v45, v45\n" \
  "v_exp_f32 v46, v46\n" \
  "v_exp_f32 v47, v47\n" \
  "v_exp_f32 v16, v16\n" \
  "v_exp_f32 v17, v17\n" \
  "v_smfmac_f32_32x32x32_bf16 v[0:15], v[64:67], v[40:47], v72\n" \
  "v_exp_f32 v18, v18\n" \
  "v_exp_f32 v19, v19\n" \
  "v_exp_f32 v20, v20\n" \
  "v_exp_f32 v21, v21\n" \
  "s_waitcnt lgkmcnt(0)\n" \
  "v_mfma_f32_32x32x16_bf16 v[32:47], v[48:51], v[68:71], 0\n" \
  "ds_read_b128 v[48:51], v73 offset:20480\n" \
  "s_mov_b64 exec, %[vm]\n" \
  "ds_read_b128 v[60:63], v74 offset:19456\n" \
  "ds_read_b128 v[64:67], v74 offset:19968\n" \
  "s_mov_b64 exec, -1\n" \
  "buffer_load_dwordx4 v[136:139], v97, %[rwo], %[swo] offen\n" \
  "s_add_u32 %[swo], %[swo], 16384\n" \
  "v_exp_f32 v22, v22\n" \
  "v_exp_f32 v23, v23\n" \
  "v_exp_f32 v24, v24\n" \
  "v_exp_f32 v25, v25\n" \
  "v_smfmac_f32_32x32x32_bf16 v[0:15], v[52:55], v[16:23], v72\n" \
  "v_exp_f32 v26, v26\n" \
  "v_exp_f32 v27, v27\n" \
  "v_exp_f32 v28, v28\n" \
  "v_exp_f32 v29, v29\n" \
  "v_exp_f32 v30, v30\n" \
  "v_exp_f32 v31, v31\n" \
  "v_exp_f32 v32, v32\n" \
  "v_exp_f32 v33, v33\n" \
  "v_smfmac_f32_32x32x32_bf16 v[0:15], v[56:59], v[24:31], v72\n" \
  "v_exp_f32 v34, v34\n" \
  "v_exp_f32 v35, v35\n" \
  "v_exp_f32 v36, v36\n" \
  "v_exp_f32 v37, v37\n" \
  "s_waitcnt lgkmcnt(0)\n" \
  "v_mfma_f32_32x32x16_bf16 v[16:31], v[48:51], v[68:71], 0\n" \
  "ds_read_b128 v[48:51], v73 offset:21504\n" \
  "s_mov_b64 exec, %[vm]\n" \
  "ds_read_b128 v[52:55], v74 offset:20480\n" \
  "ds_read_b128 v[56:59], v74 offset:20992\n" \
  "s_mov_b64 exec, -1\n" \
  "buffer_load_dwordx4 v[196:199], v98, %[refa], %[sef] offen\n" \
  "s_add_u32 %[sef], %[sef], %[sefs]\n" \
  "s_sub_u32 %[sef], %[sef], %[sefr]\n" \
  "v_exp_f32 v38, v38\n" \
  "v_exp_f32 v39, v39\n" \
  "v_exp_f32 v40, v40\n" \
  "v_exp_f32 v41, v41\n" \
  "v_smfmac_f32_32x32x32_bf16 v[0:15], v[60:63], v[32:39], v72\n" \
  "v_exp_f32 v42, v42\n" \
  "v_exp_f32 v43, v43\n" \
  "v_exp_f32 v44, v44\n" \
  "v_exp_f32 v45, v45\n" \
  "v_exp_f32 v46, v46\n" \
  "v_exp_f32 v47, v47\n" \
  "s_setprio 0\n" \
  "v_exp_f32 v16, v16\n" \
  "v_exp_f32 v17, v17\n" \
  "v_smfmac_f32_32x32x32_bf16 v[0:15], v[64:67], v[40:47], v72\n" \
  "v_exp_f32 v18, v18\n" \
  "v_exp_f32 v19, v19\n" \
  "v_exp_f32 v20, v20\n" \
  "v_exp_f32 v21, v21\n" \
  "s_waitcnt lgkmcnt(0)\n" \
  "v_mfma_f32_32x32x16_bf16 v[32:47], v[48:51], v[68:71], 0\n" \
  "ds_read_b128 v[48:51], v73 offset:22528\n" \
  "s_mov_b64 exec, %[vm]\n" \
  "ds_read_b128 v[60:63], v74 offset:21504\n" \
  "ds_read_b128 v[64:67], v74 offset:22016\n" \
  "s_mov_b64 exec, -1\n" \
  "buffer_load_dwordx4 v[140:143], v97, %[rwo], %[swo] offen\n" \
  "s_add_u32 %[swo], %[swo], 16384\n" \
  "v_exp_f32 v22, v22\n" \
  "v_exp_f32 v23, v23\n" \
  "v_exp_f32 v24, v24\n" \
  "v_exp_f32 v25, v25\n" \
  "v_smfmac_f32_32x32x32_bf16 v[0:15], v[52:55], v[16:23], v72\n" \
  "v_exp_f32 v26, v26\n" \
  "v_exp_f32 v27, v27\n" \
  "v_exp_f32 v28, v28\n" \
  "v_exp_f32 v29, v29\n" \
  "v_exp_f32 v30, v30\n" \
  "v_exp_f32 v31, v31\n" \
  "v_exp_f32 v32, v32\n" \
  "v_exp_f32 v33, v33\n" \
  "v_smfmac_f32_32x32x32_bf16 v[0:15], v[56:59], v[24:31], v72\n" \
  "v_exp_f32 v34, v34\n" \
  "v_exp_f32 v35, v35\n" \
  "v_exp_f32 v36, v36\n" \
  "v_exp_f32 v37, v37\n" \
  "s_waitcnt lgkmcnt(0)\n" \
  "v_mfma_f32_32x32x16_bf16 v[16:31], v[48:51], v[68:71], 0\n" \
  "ds_read_b128 v[48:51], v73 offset:23552\n" \
  "s_mov_b64 exec, %[vm]\n" \
  "ds_read_b128 v[52:55], v74 offset:22528\n" \
  "ds_read_b128 v[56:59], v74 offset:23040\n" \
  "s_mov_b64 exec, -1\n" \
  "s_barrier\n" \
  "v_exp_f32 v38, v38\n" \
  "v_exp_f32 v39, v39\n" \
  "v_exp_f32 v40, v40\n" \
  "v_exp_f32 v41, v41\n" \
  "v_smfmac_f32_32x32x32_bf16 v[0:15], v[60:63], v[32:39], v72\n" \
  "v_exp_f32 v42, v42\n" \
  "v_exp_f32 v43, v43\n" \
  "v_exp_f32 v44, v44\n" \
  "v_exp_f32 v45, v45\n" \
  "v_exp_f32 v46, v46\n" \
  "v_exp_f32 v47, v47\n" \
  "v_exp_f32 v16, v16\n" \
  "v_exp_f32 v17, v17\n" \
  "v_smfmac_f32_32x32x32_bf16 v[0:15], v[64:67], v[40:47], v72\n" \
  "v_exp_f32 v18, v18\n" \
  "v_exp_f32 v19, v19\n" \
  "v_exp_f32 v20, v20\n" \
  "v_exp_f32 v21, v21\n" \
  "s_waitcnt lgkmcnt(0)\n" \
  "v_mfma_f32_32x32x16_bf16 v[32:47], v[48:51], v[68:71], 0\n" \
  "ds_read_b128 v[48:51], v73 offset:24576\n" \
  "s_mov_b64 exec, %[vm]\n" \
  "ds_read_b128 v[60:63], v74 offset:23552\n" \
  "ds_read_b128 v[64:67], v74 offset:24064\n" \
  "s_mov_b64 exec, -1\n" \
  "buffer_load_dwordx4 v[144:147], v97, %[rwo], %[swo] offen\n" \
  "s_add_u32 %[swo], %[swo], 16384\n" \
  "v_exp_f32 v22, v22\n" \
  "v_exp_f32 v23, v23\n" \
  "v_exp_f32 v24, v24\n" \
  "v_exp_f32 v25, v25\n" \
  "v_smfmac_f32_32x32x32_bf16 v[0:15], v[52:55], v[16:23], v72\n" \
  "v_exp_f32 v26, v26\n" \
  "v_exp_f32 v27, v27\n" \
  "v_exp_f32 v28, v28\n" \
  "v_exp_f32 v29, v29\n" \
  "v_exp_f32 v30, v30\n" \
  "v_exp_f32 v31, v31\n" \
  "v_exp_f32 v32, v32\n" \
  "v_exp_f32 v33, v33\n" \
  "v_smfmac_f32_32x32x32_bf16 v[0:15], v[56:59], v[24:31], v72\n" \
  "v_exp_f32 v34, v34\n" \
  "v_exp_f32 v35, v35\n" \
  "v_exp_f32 v36, v36\n" \
  "v_exp_f32 v37, v37\n" \
  "s_waitcnt lgkmcnt(0)\n" \
  "v_mfma_f32_32x32x16_bf16 v[16:31], v[48:51], v[68:71], 0\n" \
  "ds_read_b128 v[48:51], v73 offset:25600\n" \
  "s_mov_b64 exec, %[vm]\n" \
  "ds_read_b128 v[52:55], v74 offset:24576\n" \
  "ds_read_b128 v[56:59], v74 offset:25088\n" \
  "s_mov_b64 exec, -1\n" \
  "v_exp_f32 v38, v38\n" \
  "v_exp_f32 v39, v39\n" \
  "v_exp_f32 v40, v40\n" \
  "v_exp_f32 v41, v41\n" \
  "v_smfmac_f32_32x32x32_bf16 v[0:15], v[60:63], v[32:39], v72\n" \
  "v_exp_f32 v42, v42\n" \
  "v_exp_f32 v43, v43\n" \
  "v_exp_f32 v44, v44\n" \
  "v_exp_f32 v45, v45\n" \
  "v_exp_f32 v46, v46\n" \
  "v_exp_f32 v47, v47\n" \
  "s_setprio 3\n" \
  "v_exp_f32 v16, v16\n" \
  "v_exp_f32 v17, v17\n" \
  "v_smfmac_f32_32x32x32_bf16 v[0:15], v[64:67], v[40:47], v72\n" \
  "v_exp_f32 v18, v18\n" \
  "v_exp_f32 v19, v19\n" \
  "v_exp_f32 v20, v20\n" \
  "v_exp_f32 v21, v21\n" \
  "s_waitcnt lgkmcnt(0)\n" \
  "v_mfma_f32_32x32x16_bf16 v[32:47], v[48:51], v[68:71], 0\n" \
  "ds_read_b128 v[48:51], v73 offset:26624\n" \
  "s_mov_b64 exec, %[vm]\n" \
  "ds_read_b128 v[60:63], v74 offset:25600\n" \
  "ds_read_b128 v[64:67], v74 offset:26112\n" \
  "s_mov_b64 exec, -1\n" \
  "s_waitcnt vmcnt(5)\n" \
  "v_max3_u32 v92, v100, v101, v102\n" \
  "v_max3_u32 v92, v92, v103, v104\n" \
  "v_max3_u32 v92, v92, v105, v106\n" \
  "v_max_u32 v92, v92, v107\n" \
  "v_cmp_eq_u32 vcc, -1, v92\n" \
  "s_nop 1\n" \
  "s_cbranch_vccnz 1014f\n" \
  "14:\n" \
  "ds_write_b128 v95, v[100:103] offset:32768\n" \
  "ds_write_b128 v96, v[104:107] offset:32768\n" \
  "buffer_load_dwordx4 v[108:111], v94, %[ws], %[k5] offen sc1\n" \
  "buffer_load_dwordx4 v[112:115], v94, %[wv2], %[k5] offen sc1\n" \
  "v_exp_f32 v22, v22\n" \
  "v_exp_f32 v23, v23\n" \
  "v_exp_f32 v24, v24\n" \
  "v_exp_f32 v25, v25\n" \
  "v_smfmac_f32_32x32x32_bf16 v[0:15], v[52:55], v[16:23], v72\n" \
  "v_exp_f32 v26, v26\n" \
  "v_exp_f32 v27, v27\n" \
  "v_exp_f32 v28, v28\n" \
  "v_exp_f32 v29, v29\n" \
  "v_exp_f32 v30, v30\n" \
  "v_exp_f32 v31, v31\n" \
  "v_exp_f32 v32, v32\n" \
  "v_exp_f32 v33, v33\n" \
  "v_smfmac_f32_32x32x32_bf16 v[0:15], v[56:59], v[24:31], v72\n" \
  "v_exp_f32 v34, v34\n" \
  "v_exp_f32 v35, v35\n" \
  "v_exp_f32 v36, v36\n" \
  "v_exp_f32 v37, v37\n" \
  "s_waitcnt lgkmcnt(0)\n" \
  "v_mfma_f32_32x32x16_bf16 v[16:31], v[48:51], v[68:71], 0\n" \
  "ds_read_b128 v[48:51], v73 offset:27648\n" \
  "s_mov_b64 exec, %[vm]\n" \
  "ds_read_b128 v[52:55], v74 offset:26624\n" \
  "ds_read_b128 v[56:59], v74 offset:27136\n" \
  "s_mov_b64 exec, -1\n" \
  "buffer_load_dwordx4 v[148:151], v97, %[rwo], %[swo] offen\n" \
  "s_add_u32 %[swo], %[swo], 16384\n" \
  "s_mov_b64 exec, 1\n" \
  "ds_add_u32 v89, v90 offset:16\n" \
  "s_mov_b64 exec, -1\n" \
  "v_exp_f32 v38, v38\n" \
  "v_exp_f32 v39, v39\n" \
  "v_exp_f32 v40, v40\n" \
  "v_exp_f32 v41, v41\n" \
  "v_smfmac_f32_32x32x32_bf16 v[0:15], v[60:63], v[32:39], v72\n" \
  "v_exp_f32 v42, v42\n" \
  "v_exp_f32 v43, v43\n" \
  "v_exp_f32 v44, v44\n" \
  "v_exp_f32 v45, v45\n" \
  "v_exp_f32 v46, v46\n" \
  "v_exp_f32 v47, v47\n" \
  "v_exp_f32 v16, v16\n" \
  "v_exp_f32 v17, v17\n" \
  "v_smfmac_f32_32x32x32_bf16 v[0:15], v[64:67], v[40:47], v72\n" \
  "v_exp_f32 v18, v18\n" \
  "v_exp_f32 v19, v19\n" \
  "v_exp_f32 v20, v20\n" \
  "v_exp_f32 v21, v21\n" \
  "s_waitcnt lgkmcnt(0)\n" \
  "v_mfma_f32_32x32x16_bf16 v[32:47], v[48:51], v[68:71], 0\n" \
  "ds_read_b128 v[48:51], v73 offset:28672\n" \
  "s_mov_b64 exec, %[vm]\n" \
  "ds_read_b128 v[60:63], v74 offset:27648\n" \
  "ds_read_b128 v[64:67], v74 offset:28160\n" \
  "s_mov_b64 exec, -1\n" \
  "buffer_load_dwordx4 v[152:155], v97, %[rwo], %[swo] offen\n" \
  "s_add_u32 %[swo], %[swo], 16384\n" \
  "v_exp_f32 v22, v22\n" \
  "v_exp_f32 v23, v23\n" \
  "v_exp_f32 v24, v24\n" \
  "v_exp_f32 v25, v25\n" \
  "v_smfmac_f32_32x32x32_bf16 v[0:15], v[52:55], v[16:23], v72\n" \
  "v_exp_f32 v26, v26\n" \
  "v_exp_f32 v27, v27\n" \
  "v_exp_f32 v28, v28\n" \
  "v_exp_f32 v29, v29\n" \
  "v_exp_f32 v30, v30\n" \
  "v_exp_f32 v31, v31\n" \
  "v_exp_f32 v32, v32\n" \
  "v_exp_f32 v33, v33\n" \
  "v_smfmac_f32_32x32x32_bf16 v[0:15], v[56:59], v[24:31], v72\n" \
  "v_exp_f32 v34, v34\n" \
  "v_exp_f32 v35, v35\n" \
  "v_exp_f32 v36, v36\n" \
  "v_exp_f32 v37, v37\n" \
  "s_waitcnt lgkmcnt(0)\n" \
  "v_mfma_f32_32x32x16_bf16 v[16:31], v[48:51], v[68:71], 0\n" \
  "ds_read_b128 v[48:51], v73 offset:29696\n" \
  "s_mov_b64 exec, %[vm]\n" \
  "ds_read_b128 v[52:55], v74 offset:28672\n" \
  "ds_read_b128 v[56:59], v74 offset:29184\n" \
  "s_mov_b64 exec, -1\n" \
  "buffer_load_dwordx4 v[200:203], v98, %[refb], %[sef] offen\n" \
  "s_add_u32 %[sef], %[sef], %[sefs]\n" \
  "v_exp_f32 v38, v38\n" \
  "v_exp_f32 v39, v39\n" \
  "v_exp_f32 v40, v40\n" \
  "v_exp_f32 v41, v41\n" \
  "v_smfmac_f32_32x32x32_bf16 v[0:15], v[60:63], v[32:39], v72\n" \
  "v_exp_f32 v42, v42\n" \
  "v_exp_f32 v43, v43\n" \
  "v_exp_f32 v44, v44\n" \
  "v_exp_f32 v45, v45\n" \
  "v_exp_f32 v46, v46\n" \
  "v_exp_f32 v47, v47\n" \
  "s_setprio 2\n" \
  "v_exp_f32 v16, v16\n" \
  "v_exp_f32 v17, v17\n" \
  "v_smfmac_f32_32x32x32_bf16 v[0:15], v[64:67], v[40:47], v72\n" \
  "v_exp_f32 v18, v18\n" \
  "v_exp_f32 v19, v19\n" \
  "v_exp_f32 v20, v20\n" \
  "v_exp_f32 v21, v21\n" \
  "s_waitcnt lgkmcnt(0)\n" \
  "v_mfma_f32_32x32x16_bf16 v[32:47], v[48:51], v[68:71], 0\n" \
  "ds_read_b128 v[48:51], v73 offset:30720\n" \
  "s_mov_b64 exec, %[vm]\n" \
  "ds_read_b128 v[60:63], v74 offset:29696\n" \
  "ds_read_b128 v[64:67], v74 offset:30208\n" \
  "s_mov_b64 exec, -1\n" \
  "buffer_load_dwordx4 v[156:159], v97, %[rwo], %[swo] offen\n" \
  "s_add_u32 %[swo], %[swo], 16384\n" \
  "v_exp_f32 v22, v22\n" \
  "v_exp_f32 v23, v23\n" \
  "v_exp_f32 v24, v24\n" \
  "v_exp_f32 v25, v25\n" \
  "v_smfmac_f32_32x32x32_bf16 v[0:15], v[52:55], v[16:23], v72\n" \
  "v_exp_f32 v26, v26\n" \
  "v_exp_f32 v27, v27\n" \
  "v_exp_f32 v28, v28\n" \
  "v_exp_f32 v29, v29\n" \
  "v_exp_f32 v30, v30\n" \
  "v_exp_f32 v31, v31\n" \
  "v_exp_f32 v32, v32\n" \
  "v_exp_f32 v33, v33\n" \
  "v_smfmac_f32_32x32x32_bf16 v[0:15], v[56:59], v[24:31], v72\n" \
  "v_exp_f32 v34, v34\n" \
  "v_exp_f32 v35, v35\n" \
  "v_exp_f32 v36, v36\n" \
  "v_exp_f32 v37, v37\n" \
  "s_waitcnt lgkmcnt(0)\n" \
  "v_mfma_f32_32x32x16_bf16 v[16:31], v[48:51], v[68:71], 0\n" \
  "ds_read_b128 v[48:51], v73 offset:31744\n" \
  "s_mov_b64 exec, %[vm]\n" \
  "ds_read_b128 v[52:55], v74 offset:30720\n" \
  "ds_read_b128 v[56:59], v74 offset:31232\n" \
  "s_mov_b64 exec, -1\n" \
  "ds_read_b32 v93, v89 offset:16\n" \
  "v_exp_f32 v38, v38\n" \
  "v_exp_f32 v39, v39\n" \
  "v_exp_f32 v40, v40\n" \
  "v_exp_f32 v41, v41\n" \
  "v_smfmac_f32_32x32x32_bf16 v[0:15], v[60:63], v[32:39], v72\n" \
  "v_exp_f32 v42, v42\n" \
  "v_exp_f32 v43, v43\n" \
  "v_exp_f32 v44, v44\n" \
  "v_exp_f32 v45, v45\n" \
  "v_exp_f32 v46, v46\n" \
  "v_exp_f32 v47, v47\n" \
  "v_exp_f32 v16, v16\n" \
  "v_exp_f32 v17, v17\n" \
  "v_smfmac_f32_32x32x32_bf16 v[0:15], v[64:67], v[40:47], v72\n" \
  "v_exp_f32 v18, v18\n" \
  "v_exp_f32 v19, v19\n" \
  "v_exp_f32 v20, v20\n" \
  "v_exp_f32 v21, v21\n" \
  "s_waitcnt lgkmcnt(0)\n" \
  "v_mfma_f32_32x32x16_bf16 v[32:47], v[48:51], v[68:71], 0\n" \
  "v_readfirstlane_b32 %[sc], v93\n" \
  "s_nop 0\n" \
  "s_cmp_lt_u32 %[sc], 8\n" \
  "s_cbranch_scc1 1015f\n" \
  "15:\n" \
  "ds_read_b128 v[48:51], v73 offset:32768\n" \
  "s_mov_b64 exec, %[vm]\n" \
  "ds_read_b128 v[60:63], v74 offset:31744\n" \
  "ds_read_b128 v[64:67], v74 offset:32256\n" \
  "s_mov_b64 exec, -1\n" \
  "buffer_load_dwordx4 v[160:163], v97, %[rwo], %[swo] offen\n" \
  "s_add_u32 %[swo], %[swo], 16384\n" \
  "v_exp_f32 v22, v22\n" \
  "v_exp_f32 v23, v23\n" \
  "v_exp_f32 v24, v24\n" \
  "v_exp_f32 v25, v25\n" \
  "v_smfmac_f32_32x32x32_bf16 v[0:15], v[52:55], v[16:23], v72\n" \
  "v_exp_f32 v26, v26\n" \
  "v_exp_f32 v27, v27\n" \
  "v_exp_f32 v28, v28\n" \
  "v_exp_f32 v29, v29\n" \
  "v_exp_f32 v30, v30\n" \
  "v_exp_f32 v31, v31\n" \
  "v_exp_f32 v32, v32\n" \
  "v_exp_f32 v33, v33\n" \
  "v_smfmac_f32_32x32x32_bf16 v[0:15], v[56:59], v[24:31], v72\n" \
  "v_exp_f32 v34, v34\n" \
  "v_exp_f32 v35, v35\n" \
  "v_exp_f32 v36, v36\n" \
  "v_exp_f32 v37, v37\n" \
  "s_waitcnt lgkmcnt(0)\n" \
  "v_mfma_f32_32x32x16_bf16 v[16:31], v[48:51], v[68:71], 0\n" \
  "ds_read_b128 v[48:51], v73 offset:33792\n" \
  "s_mov_b64 exec, %[vm]\n" \
  "ds_read_b128 v[52:55], v75 offset:0\n" \
  "ds_read_b128 v[56:59], v75 offset:512\n" \
  "s_mov_b64 exec, -1\n" \
  "v_exp_f32 v38, v38\n" \
  "v_exp_f32 v39, v39\n" \
  "v_exp_f32 v40, v40\n" \
  "v_exp_f32 v41, v41\n" \
  "v_smfmac_f32_32x32x32_bf16 v[0:15], v[60:63], v[32:39], v72\n" \
  "v_exp_f32 v42, v42\n" \
  "v_exp_f32 v43, v43\n" \
  "v_exp_f32 v44, v44\n" \
  "v_exp_f32 v45, v45\n" \
  "v_exp_f32 v46, v46\n" \
  "v_exp_f32 v47, v47\n" \
  "s_setprio 1\n" \
  "v_exp_f32 v16, v16\n" \
  "v_exp_f32 v17, v17\n" \
  "v_smfmac_f32_32x32x32_bf16 v[0:15], v[64:67], v[40:47], v72\n" \
  "v_exp_f32 v18, v18\n" \
  "v_exp_f32 v19, v19\n" \
  "v_exp_f32 v20, v20\n" \
  "v_exp_f32 v21, v21\n" \
  "s_waitcnt lgkmcnt(0)\n" \
  "v_mfma_f32_32x32x16_bf16 v[32:47], v[48:51], v[68:71], 0\n" \
  "ds_read_b128 v[48:51], v73 offset:34816\n" \
  "s_mov_b64 exec, %[vm]\n" \
  "ds_read_b128 v[60:63], v75 offset:1024\n" \
  "ds_read_b128 v[64:67], v75 offset:1536\n" \
  "s_mov_b64 exec, -1\n" \
  "s_waitcnt vmcnt(5)\n" \
  "v_max3_u32 v92, v108, v109, v110\n" \
  "v_max3_u32 v92, v92, v111, v112\n" \
  "v_max3_u32 v92, v92, v113, v114\n" \
  "v_max_u32 v92, v92, v115\n" \
  "v_cmp_eq_u32 vcc, -1, v92\n" \
  "s_nop 1\n" \
  "s_cbranch_vccnz 1016f\n" \
  "16:\n" \
  "ds_write_b128 v95, v[108:111] offset:40960\n" \
  "ds_write_b128 v96, v[112:115] offset:40960\n" \
  "buffer_load_dwordx4 v[100:103], v94, %[ws], %[k6] offen sc1\n" \
  "buffer_load_dwordx4 v[104:107], v94, %[wv2], %[k6] offen sc1\n" \
  "v_exp_f32 v22, v22\n" \
  "v_exp_f32 v23, v23\n" \
  "v_exp_f32 v24, v24\n" \
  "v_exp_f32 v25, v25\n" \
  "v_smfmac_f32_32x32x32_bf16 v[0:15], v[52:55], v[16:23], v72\n" \
  "v_exp_f32 v26, v26\n" \
  "v_exp_f32 v27, v27\n" \
  "v_exp_f32 v28, v28\n" \
  "v_exp_f32 v29, v29\n" \
  "v_exp_f32 v30, v30\n" \
  "v_exp_f32 v31, v31\n" \
  "v_exp_f32 v32, v32\n" \
  "v_exp_f32 v33, v33\n" \
  "v_smfmac_f32_32x32x32_bf16 v[0:15], v[56:59], v[24:31], v72\n" \
  "v_exp_f32 v34, v34\n" \
  "v_exp_f32 v35, v35\n" \
  "v_exp_f32 v36, v36\n" \
  "v_exp_f32 v37, v37\n" \
  "s_waitcnt lgkmcnt(0)\n" \
  "v_mfma_f32_32x32x16_bf16 v[16:31], v[48:51], v[68:71], 0\n" \
  "ds_read_b128 v[48:51], v73 offset:35840\n" \
  "s_mov_b64 exec, %[vm]\n" \
  "ds_read_b128 v[52:55], v75 offset:2048\n" \
  "ds_read_b128 v[56:59], v75 offset:2560\n" \
  "s_mov_b64 exec, -1\n" \
  "buffer_load_dwordx4 v[164:167], v97, %[rwo], %[swo] offen\n" \
  "s_add_u32 %[swo], %[swo], 16384\n" \
  "v_exp_f32 v38, v38\n" \
  "v_exp_f32 v39, v39\n" \
  "v_exp_f32 v40, v40\n" \
  "v_exp_f32 v41, v41\n" \
  "v_smfmac_f32_32x32x32_bf16 v[0:15], v[60:63], v[32:39], v72\n" \
  "v_exp_f32 v42, v42\n" \
  "v_exp_f32 v43, v43\n" \
  "v_exp_f32 v44, v44\n" \
  "v_exp_f32 v45, v45\n" \
  "v_exp_f32 v46, v46\n" \
  "v_exp_f32 v47, v47\n" \
  "v_exp_f32 v16, v16\n" \
  "v_exp_f32 v17, v17\n" \
  "v_smfmac_f32_32x32x32_bf16 v[0:15], v[64:67], v[40:47], v72\n" \
  "v_exp_f32 v18, v18\n" \
  "v_exp_f32 v19, v19\n" \
  "v_exp_f32 v20, v20\n" \
  "v_exp_f32 v21, v21\n" \
  "s_waitcnt lgkmcnt(0)\n" \
  "v_mfma_f32_32x32x16_bf16 v[32:47], v[48:51], v[68:71], 0\n" \
  "ds_read_b128 v[48:51], v73 offset:36864\n" \
  "s_mov_b64 exec, %[vm]\n" \
  "ds_read_b128 v[60:63], v75 offset:3072\n" \
  "ds_read_b128 v[64:67], v75 offset:3584\n" \
  "s_mov_b64 exec, -1\n" \
  "buffer_load_dwordx4 v[168:171], v97, %[rwo], %[swo] offen\n" \
  "s_add_u32 %[swo], %[swo], 16384\n" \
  "v_exp_f32 v22, v22\n" \
  "v_exp_f32 v23, v23\n" \
  "v_exp_f32 v24, v24\n" \
  "v_exp_f32 v25, v25\n" \
  "v_smfmac_f32_32x32x32_bf16 v[0:15], v[52:55], v[16:23], v72\n" \
  "v_exp_f32 v26, v26\n" \
  "v_exp_f32 v27, v27\n" \
  "v_exp_f32 v28, v28\n" \
  "v_exp_f32 v29, v29\n" \
  "v_exp_f32 v30, v30\n" \
  "v_exp_f32 v31, v31\n" \
  "v_exp_f32 v32, v32\n" \
  "v_exp_f32 v33, v33\n" \
  "v_smfmac_f32_32x32x32_bf16 v[0:15], v[56:59], v[24:31], v72\n" \
  "v_exp_f32 v34, v34\n" \
  "v_exp_f32 v35, v35\n" \
  "v_exp_f32 v36, v36\n" \
  "v_exp_f32 v37, v37\n" \
  "s_waitcnt lgkmcnt(0)\n" \
  "v_mfma_f32_32x32x16_bf16 v[16:31], v[48:51], v[68:71], 0\n" \
  "ds_read_b128 v[48:51], v73 offset:37888\n" \
  "s_mov_b64 exec, %[vm]\n" \
  "ds_read_b128 v[52:55], v75 offset:4096\n" \
  "ds_read_b128 v[56:59], v75 offset:4608\n" \
  "s_mov_b64 exec, -1\n" \
  "buffer_load_dwordx4 v[204:207], v98, %[refb], %[sef] offen\n" \
  "s_add_u32 %[sef], %[sef], %[sefs]\n" \
  "v_exp_f32 v38, v38\n" \
  "v_exp_f32 v39, v39\n" \
  "v_exp_f32 v40, v40\n" \
  "v_exp_f32 v41, v41\n" \
  "v_smfmac_f32_32x32x32_bf16 v[0:15], v[60:63], v[32:39], v72\n" \
  "v_exp_f32 v42, v42\n" \
  "v_exp_f32 v43, v43\n" \
  "v_exp_f32 v44, v44\n" \
  "v_exp_f32 v45, v45\n" \
  "v_exp_f32 v46, v46\n" \
  "v_exp_f32 v47, v47\n" \
  "s_setprio 0\n" \
  "v_exp_f32 v16, v16\n" \
  "v_exp_f32 v17, v17\n" \
  "v_smfmac_f32_32x32x32_bf16 v[0:15], v[64:67], v[40:47], v72\n" \
  "v_exp_f32 v18, v18\n" \
  "v_exp_f32 v19, v19\n" \
  "v_exp_f32 v20, v20\n" \
  "v_exp_f32 v21, v21\n" \
  "s_waitcnt lgkmcnt(0)\n" \
  "v_mfma_f32_32x32x16_bf16 v[32:47], v[48:51], v[68:71], 0\n" \
  "ds_read_b128 v[48:51], v73 offset:38912\n" \
  "s_mov_b64 exec, %[vm]\n" \
  "ds_read_b128 v[60:63], v75 offset:5120\n" \
  "ds_read_b128 v[64:67], v75 offset:5632\n" \
  "s_mov_b64 exec, -1\n" \
  "buffer_load_dwordx4 v[172:175], v97, %[rwo], %[swo] offen\n" \
  "s_add_u32 %[swo], %[swo], 16384\n" \
  "v_exp_f32 v22, v22\n" \
  "v_exp_f32 v23, v23\n" \
  "v_exp_f32 v24, v24\n" \
  "v_exp_f32 v25, v25\n" \
  "v_smfmac_f32_32x32x32_bf16 v[0:15], v[52:55], v[16:23], v72\n" \
  "v_exp_f32 v26, v26\n" \
  "v_exp_f32 v27, v27\n" \
  "v_exp_f32 v28, v28\n" \
  "v_exp_f32 v29, v29\n" \
  "v_exp_f32 v30, v30\n" \
  "v_exp_f32 v31, v31\n" \
  "v_exp_f32 v32, v32\n" \
  "v_exp_f32 v33, v33\n" \
  "v_smfmac_f32_32x32x32_bf16 v[0:15], v[56:59], v[24:31], v72\n" \
  "v_exp_f32 v34, v34\n" \
  "v_exp_f32 v35, v35\n" \
  "v_exp_f32 v36, v36\n" \
  "v_exp_f32 v37, v37\n" \
  "s_waitcnt lgkmcnt(0)\n" \
  "v_mfma_f32_32x32x16_bf16 v[16:31], v[48:51], v[68:71], 0\n" \
  "ds_read_b128 v[48:51], v73 offset:39936\n" \
  "s_mov_b64 exec, %[vm]\n" \
  "ds_read_b128 v[52:55], v75 offset:6144\n" \
  "ds_read_b128 v[56:59], v75 offset:6656\n" \
  "s_mov_b64 exec, -1\n" \
  "s_barrier\n" \
  "v_exp_f32 v38, v38\n" \
  "v_exp_f32 v39, v39\n" \
  "v_exp_f32 v40, v40\n" \
  "v_exp_f32 v41, v41\n" \
  "v_smfmac_f32_32x32x32_bf16 v[0:15], v[60:63], v[32:39], v72\n" \
  "v_exp_f32 v42, v42\n" \
  "v_exp_f32 v43, v43\n" \
  "v_exp_f32 v44, v44\n" \
  "v_exp_f32 v45, v45\n" \
  "v_exp_f32 v46, v46\n" \
  "v_exp_f32 v47, v47\n" \
  "v_exp_f32 v16, v16\n" \
  "v_exp_f32 v17, v17\n" \
  "v_smfmac_f32_32x32x32_bf16 v[0:15], v[64:67], v[40:47], v72\n" \
  "v_exp_f32 v18, v18\n" \
  "v_exp_f32 v19, v19\n" \
  "v_exp_f32 v20, v20\n" \
  "v_exp_f32 v21, v21\n" \
  "s_waitcnt lgkmcnt(0)\n" \
  "v_mfma_f32_32x32x16_bf16 v[32:47], v[48:51], v[68:71], 0\n" \
  "ds_read_b128 v[48:51], v73 offset:40960\n" \
  "s_mov_b64 exec, %[vm]\n" \
  "ds_read_b128 v[60:63], v75 offset:7168\n" \
  "ds_read_b128 v[64:67], v75 offset:7680\n" \
  "s_mov_b64 exec, -1\n" \
  "buffer_load_dwordx4 v[176:179], v97, %[rwo], %[swo] offen\n" \
  "s_add_u32 %[swo], %[swo], 16384\n" \
  "v_exp_f32 v22, v22\n" \
  "v_exp_f32 v23, v23\n" \
  "v_exp_f32 v24, v24\n" \
  "v_exp_f32 v25, v25\n" \
  "v_smfmac_f32_32x32x32_bf16 v[0:15], v[52:55], v[16:23], v72\n" \
  "v_exp_f32 v26, v26\n" \
  "v_exp_f32 v27, v27\n" \
  "v_exp_f32 v28, v28\n" \
  "v_exp_f32 v29, v29\n" \
  "v_exp_f32 v30, v30\n" \
  "v_exp_f32 v31, v31\n" \
  "v_exp_f32 v32, v32\n" \
  "v_exp_f32 v33, v33\n" \
  "v_smfmac_f32_32x32x32_bf16 v[0:15], v[56:59], v[24:31], v72\n" \
  "v_exp_f32 v34, v34\n" \
  "v_exp_f32 v35, v35\n" \
  "v_exp_f32 v36, v36\n" \
  "v_exp_f32 v37, v37\n" \
  "s_waitcnt lgkmcnt(0)\n" \
  "v_mfma_f32_32x32x16_bf16 v[16:31], v[48:51], v[68:71], 0\n" \
  "ds_read_b128 v[48:51], v73 offset:41984\n" \
  "s_mov_b64 exec, %[vm]\n" \
  "ds_read_b128 v[52:55], v75 offset:8192\n" \
  "ds_read_b128 v[56:59], v75 offset:8704\n" \
  "s_mov_b64 exec, -1\n" \
  "s_cmp_eq_u32 %[wv], 0\n" \
  "s_cbranch_scc1 81f\n" \
  "80:\n" \
  "v_exp_f32 v38, v38\n" \
  "v_exp_f32 v39, v39\n" \
  "v_exp_f32 v40, v40\n" \
  "v_exp_f32 v41, v41\n" \
  "v_smfmac_f32_32x32x32_bf16 v[0:15], v[60:63], v[32:39], v72\n" \
  "v_exp_f32 v42, v42\n" \
  "v_exp_f32 v43, v43\n" \
  "v_exp_f32 v44, v44\n" \
  "v_exp_f32 v45, v45\n" \
  "v_exp_f32 v46, v46\n" \
  "v_exp_f32 v47, v47\n" \
  "s_setprio 3\n" \
  "v_exp_f32 v16, v16\n" \
  "v_exp_f32 v17, v17\n" \
  "v_smfmac_f32_32x32x32_bf16 v[0:15], v[64:67], v[40:47], v72\n" \
  "v_exp_f32 v18, v18\n" \
  "v_exp_f32 v19, v19\n" \
  "v_exp_f32 v20, v20\n" \
  "v_exp_f32 v21, v21\n" \
  "s_waitcnt lgkmcnt(0)\n" \
  "v_mfma_f32_32x32x16_bf16 v[32:47], v[48:51], v[68:71], 0\n" \
  "ds_read_b128 v[48:51], v73 offset:43008\n" \
  "s_mov_b64 exec, %[vm]\n" \
  "ds_read_b128 v[60:63], v75 offset:9216\n" \
  "ds_read_b128 v[64:67], v75 offset:9728\n" \
  "s_mov_b64 exec, -1\n" \
  "s_waitcnt vmcnt(5)\n" \
  "v_max3_u32 v92, v100, v101, v102\n" \
  "v_max3_u32 v92, v92, v103, v104\n" \
  "v_max3_u32 v92, v92, v105, v106\n" \
  "v_max_u32 v92, v92, v107\n" \
  "v_cmp_eq_u32 vcc, -1, v92\n" \
  "s_nop 1\n" \
  "s_cbranch_vccnz 1017f\n" \
  "17:\n" \
  "ds_write_b128 v95, v[100:103] offset:49152\n" \
  "ds_write_b128 v96, v[104:107] offset:49152\n" \
  "buffer_load_dwordx4 v[108:111], v94, %[ws], %[k7] offen sc1\n" \
  "buffer_load_dwordx4 v[112:115], v94, %[wv2], %[k7] offen sc1\n" \
  "v_exp_f32 v22, v22\n" \
  "v_exp_f32 v23, v23\n" \
  "v_exp_f32 v24, v24\n" \
  "v_exp_f32 v25, v25\n" \
  "v_smfmac_f32_32x32x32_bf16 v[0:15], v[52:55], v[16:23], v72\n" \
  "v_exp_f32 v26, v26\n" \
  "v_exp_f32 v27, v27\n" \
  "v_exp_f32 v28, v28\n" \
  "v_exp_f32 v29, v29\n" \
  "v_exp_f32 v30, v30\n" \
  "v_exp_f32 v31, v31\n" \
  "v_exp_f32 v32, v32\n" \
  "v_exp_f32 v33, v33\n" \
  "v_smfmac_f32_32x32x32_bf16 v[0:15], v[56:59], v[24:31], v72\n" \
  "v_exp_f32 v34, v34\n" \
  "v_exp_f32 v35, v35\n" \
  "v_exp_f32 v36, v36\n" \
  "v_exp_f32 v37, v37\n" \
  "s_waitcnt lgkmcnt(0)\n" \
  "v_mfma_f32_32x32x16_bf16 v[16:31], v[48:51], v[68:71], 0\n" \
  "ds_read_b128 v[48:51], v73 offset:44032\n" \
  "s_mov_b64 exec, %[vm]\n" \
  "ds_read_b128 v[52:55], v75 offset:10240\n" \
  "ds_read_b128 v[56:59], v75 offset:10752\n" \
  "s_mov_b64 exec, -1\n" \
  "buffer_load_dwordx4 v[180:183], v97, %[rwo], %[swo] offen\n" \
  "s_add_u32 %[swo], %[swo], 16384\n" \
  "s_mov_b64 exec, 1\n" \
  "ds_add_u32 v89, v90 offset:24\n" \
  "s_mov_b64 exec, -1\n" \
  "v_exp_f32 v38, v38\n" \
  "v_exp_f32 v39, v39\n" \
  "v_exp_f32 v40, v40\n" \
  "v_exp_f32 v41, v41\n" \
  "v_smfmac_f32_32x32x32_bf16 v[0:15], v[60:63], v[32:39], v72\n" \
  "v_exp_f32 v42, v42\n" \
  "v_exp_f32 v43, v43\n" \
  "v_exp_f32 v44, v44\n" \
  "v_exp_f32 v45, v45\n" \
  "v_exp_f32 v46, v46\n" \
  "v_exp_f32 v47, v47\n" \
  "v_exp_f32 v16, v16\n" \
  "v_exp_f32 v17, v17\n" \
  "v_smfmac_f32_32x32x32_bf16 v[0:15], v[64:67], v[40:47], v72\n" \
  "v_exp_f32 v18, v18\n" \
  "v_exp_f32 v19, v19\n" \
  "v_exp_f32 v20, v20\n" \
  "v_exp_f32 v21, v21\n" \
  "s_waitcnt lgkmcnt(0)\n" \
  "v_mfma_f32_32x32x16_bf16 v[32:47], v[48:51], v[68:71], 0\n" \
  "ds_read_b128 v[48:51], v73 offset:45056\n" \
  "s_mov_b64 exec, %[vm]\n" \
  "ds_read_b128 v[60:63], v75 offset:11264\n" \
  "ds_read_b128 v[64:67], v75 offset:11776\n" \
  "s_mov_b64 exec, -1\n" \
  "buffer_load_dwordx4 v[184:187], v97, %[rwo], %[swo] offen\n" \
  "s_add_u32 %[swo], %[swo], 16384\n" \
  "v_exp_f32 v22, v22\n" \
  "v_exp_f32 v23, v23\n" \
  "v_exp_f32 v24, v24\n" \
  "v_exp_f32 v25, v25\n" \
  "v_smfmac_f32_32x32x32_bf16 v[0:15], v[52:55], v[16:23], v72\n" \
  "v_exp_f32 v26, v26\n" \
  "v_exp_f32 v27, v27\n" \
  "v_exp_f32 v28, v28\n" \
  "v_exp_f32 v29, v29\n" \
  "v_exp_f32 v30, v30\n" \
  "v_exp_f32 v31, v31\n" \
  "v_exp_f32 v32, v32\n" \
  "v_exp_f32 v33, v33\n" \
  "v_smfmac_f32_32x32x32_bf16 v[0:15], v[56:59], v[24:31], v72\n" \
  "v_exp_f32 v34, v34\n" \
  "v_exp_f32 v35, v35\n" \
  "v_exp_f32 v36, v36\n" \
  "v_exp_f32 v37, v37\n" \
  "s_waitcnt lgkmcnt(0)\n" \
  "v_mfma_f32_32x32x16_bf16 v[16:31], v[48:51], v[68:71], 0\n" \
  "ds_read_b128 v[48:51], v73 offset:46080\n" \
  "s_mov_b64 exec, %[vm]\n" \
  "ds_read_b128 v[52:55], v75 offset:12288\n" \
  "ds_read_b128 v[56:59], v75 offset:12800\n" \
  "s_mov_b64 exec, -1\n" \
  "buffer_load_dwordx4 v[208:211], v98, %[refc], %[sef] offen\n" \
  "s_add_u32 %[sef], %[sef], %[sefs]\n" \
  "v_exp_f32 v38, v38\n" \
  "v_exp_f32 v39, v39\n" \
  "v_exp_f32 v40, v40\n" \
  "v_exp_f32 v41, v41\n" \
  "v_smfmac_f32_32x32x32_bf16 v[0:15], v[60:63], v[32:39], v72\n" \
  "v_exp_f32 v42, v42\n" \
  "v_exp_f32 v43, v43\n" \
  "v_exp_f32 v44, v44\n" \
  "v_exp_f32 v45, v45\n" \
  "v_exp_f32 v46, v46\n" \
  "v_exp_f32 v47, v47\n" \
  "s_setprio 2\n" \
  "v_exp_f32 v16, v16\n" \
  "v_exp_f32 v17, v17\n" \
  "v_smfmac_f32_32x32x32_bf16 v[0:15], v[64:67], v[40:47], v72\n" \
  "v_exp_f32 v18, v18\n" \
  "v_exp_f32 v19, v19\n" \
  "v_exp_f32 v20, v20\n" \
  "v_exp_f32 v21, v21\n" \
  "s_waitcnt lgkmcnt(0)\n" \
  "v_mfma_f32_32x32x16_bf16 v[32:47], v[48:51], v[68:71], 0\n" \
  "ds_read_b128 v[48:51], v73 offset:47104\n" \
  "s_mov_b64 exec, %[vm]\n" \
  "ds_read_b128 v[60:63], v75 offset:13312\n" \
  "ds_read_b128 v[64:67], v75 offset:13824\n" \
  "s_mov_b64 exec, -1\n" \
  "buffer_load_dwordx4 v[188:191], v97, %[rwo], %[swo] offen\n" \
  "s_add_u32 %[swo], %[swo], 16384\n" \
  "v_exp_f32 v22, v22\n" \
  "v_exp_f32 v23, v23\n" \
  "v_exp_f32 v24, v24\n" \
  "v_exp_f32 v25, v25\n" \
  "v_smfmac_f32_32x32x32_bf16 v[0:15], v[52:55], v[16:23], v72\n" \
  "v_exp_f32 v26, v26\n" \
  "v_exp_f32 v27, v27\n" \
  "v_exp_f32 v28, v28\n" \
  "v_exp_f32 v29, v29\n" \
  "v_exp_f32 v30, v30\n" \
  "v_exp_f32 v31, v31\n" \
  "v_exp_f32 v32, v32\n" \
  "v_exp_f32 v33, v33\n" \
  "v_smfmac_f32_32x32x32_bf16 v[0:15], v[56:59], v[24:31], v72\n" \
  "v_exp_f32 v34, v34\n" \
  "v_exp_f32 v35, v35\n" \
  "v_exp_f32 v36, v36\n" \
  "v_exp_f32 v37, v37\n" \
  "s_waitcnt lgkmcnt(0)\n" \
  "v_mfma_f32_32x32x16_bf16 v[16:31], v[48:51], v[68:71], 0\n" \
  "ds_read_b128 v[48:51], v73 offset:48128\n" \
  "s_mov_b64 exec, %[vm]\n" \
  "ds_read_b128 v[52:55], v75 offset:14336\n" \
  "ds_read_b128 v[56:59], v75 offset:14848\n" \
  "s_mov_b64 exec, -1\n" \
  "ds_read_b32 v93, v89 offset:24\n" \
  "v_exp_f32 v38, v38\n" \
  "v_exp_f32 v39, v39\n" \
  "v_exp_f32 v40, v40\n" \
  "v_exp_f32 v41, v41\n" \
  "v_smfmac_f32_32x32x32_bf16 v[0:15], v[60:63], v[32:39], v72\n" \
  "v_exp_f32 v42, v42\n" \
  "v_exp_f32 v43, v43\n" \
  "v_exp_f32 v44, v44\n" \
  "v_exp_f32 v45, v45\n" \
  "v_exp_f32 v46, v46\n" \
  "v_exp_f32 v47, v47\n" \
  "v_exp_f32 v16, v16\n" \
  "v_exp_f32 v17, v17\n" \
  "v_smfmac_f32_32x32x32_bf16 v[0:15], v[64:67], v[40:47], v72\n" \
  "v_exp_f32 v18, v18\n" \
  "v_exp_f32 v19, v19\n" \
  "v_exp_f32 v20, v20\n" \
  "v_exp_f32 v21, v21\n" \
  "s_waitcnt lgkmcnt(0)\n" \
  "v_mfma_f32_32x32x16_bf16 v[32:47], v[48:51], v[68:71], 0\n" \
  "v_readfirstlane_b32 %[sc], v93\n" \
  "s_nop 0\n" \
  "s_cmp_lt_u32 %[sc], 8\n" \
  "s_cbranch_scc1 1018f\n" \
  "18:\n" \
  "ds_read_b128 v[48:51], v73 offset:49152\n" \
  "s_mov_b64 exec, %[vm]\n" \
  "ds_read_b128 v[60:63], v75 offset:15360\n" \
  "ds_read_b128 v[64:67], v75 offset:15872\n" \
  "s_mov_b64 exec, -1\n" \
  "buffer_load_dwordx2 v[220:221], v123, %[rx], %[sxr] offen\n" \
  "v_exp_f32 v22, v22\n" \
  "v_exp_f32 v23, v23\n" \
  "v_exp_f32 v24, v24\n" \
  "v_exp_f32 v25, v25\n" \
  "v_smfmac_f32_32x32x32_bf16 v[0:15], v[52:55], v[16:23], v72\n" \
  "v_exp_f32 v26, v26\n" \
  "v_exp_f32 v27, v27\n" \
  "v_exp_f32 v28, v28\n" \
  "v_exp_f32 v29, v29\n" \
  "v_exp_f32 v30, v30\n" \
  "v_exp_f32 v31, v31\n" \
  "v_exp_f32 v32, v32\n" \
  "v_exp_f32 v33, v33\n" \
  "v_smfmac_f32_32x32x32_bf16 v[0:15], v[56:59], v[24:31], v72\n" \
  "v_exp_f32 v34, v34\n" \
  "v_exp_f32 v35, v35\n" \
  "v_exp_f32 v36, v36\n" \
  "v_exp_f32 v37, v37\n" \
  "s_waitcnt lgkmcnt(0)\n" \
  "v_mfma_f32_32x32x16_bf16 v[16:31], v[48:51], v[68:71], 0\n" \
  "ds_read_b128 v[48:51], v73 offset:50176\n" \
  "s_mov_b64 exec, %[vm]\n" \
  "ds_read_b128 v[52:55], v75 offset:16384\n" \
  "ds_read_b128 v[56:59], v75 offset:16896\n" \
  "s_mov_b64 exec, -1\n" \
  "v_exp_f32 v38, v38\n" \
  "v_exp_f32 v39, v39\n" \
  "v_exp_f32 v40, v40\n" \
  "v_exp_f32 v41, v41\n" \
  "v_smfmac_f32_32x32x32_bf16 v[0:15], v[60:63], v[32:39], v72\n" \
  "v_exp_f32 v42, v42\n" \
  "v_exp_f32 v43, v43\n" \
  "v_exp_f32 v44, v44\n" \
  "v_exp_f32 v45, v45\n" \
  "v_exp_f32 v46, v46\n" \
  "v_exp_f32 v47, v47\n" \
  "s_setprio 1\n" \
  "v_exp_f32 v16, v16\n" \
  "v_exp_f32 v17, v17\n" \
  "v_smfmac_f32_32x32x32_bf16 v[0:15], v[64:67], v[40:47], v72\n" \
  "v_exp_f32 v18, v18\n" \
  "v_exp_f32 v19, v19\n" \
  "v_exp_f32 v20, v20\n" \
  "v_exp_f32 v21, v21\n" \
  "s_waitcnt lgkmcnt(0)\n" \
  "v_mfma_f32_32x32x16_bf16 v[32:47], v[48:51], v[68:71], 0\n" \
  "ds_read_b128 v[48:51], v73 offset:51200\n" \
  "s_mov_b64 exec, %[vm]\n" \
  "ds_read_b128 v[60:63], v75 offset:17408\n" \
  "ds_read_b128 v[64:67], v75 offset:17920\n" \
  "s_mov_b64 exec, -1\n" \
  "s_waitcnt vmcnt(5)\n" \
  "v_max3_u32 v92, v108, v109, v110\n" \
  "v_max3_u32 v92, v92, v111, v112\n" \
  "v_max3_u32 v92, v92, v113, v114\n" \
  "v_max_u32 v92, v92, v115\n" \
  "v_cmp_eq_u32 vcc, -1, v92\n" \
  "s_nop 1\n" \
  "s_cbranch_vccnz 1019f\n" \
  "19:\n" \
  "ds_write_b128 v95, v[108:111] offset:57344\n" \
  "ds_write_b128 v96, v[112:115] offset:57344\n" \
  "v_exp_f32 v22, v22\n" \
  "v_exp_f32 v23, v23\n" \
  "v_exp_f32 v24, v24\n" \
  "v_exp_f32 v25, v25\n" \
  "v_smfmac_f32_32x32x32_bf16 v[0:15], v[52:55], v[16:23], v72\n" \
  "v_exp_f32 v26, v26\n" \
  "v_exp_f32 v27, v27\n" \
  "v_exp_f32 v28, v28\n" \
  "v_exp_f32 v29, v29\n" \
  "v_exp_f32 v30, v30\n" \
  "v_exp_f32 v31, v31\n" \
  "v_exp_f32 v32, v32\n" \
  "v_exp_f32 v33, v33\n" \
  "v_smfmac_f32_32x32x32_bf16 v[0:15], v[56:59], v[24:31], v72\n" \
  "v_exp_f32 v34, v34\n" \
  "v_exp_f32 v35, v35\n" \
  "v_exp_f32 v36, v36\n" \
  "v_exp_f32 v37, v37\n" \
  "s_waitcnt lgkmcnt(0)\n" \
  "v_mfma_f32_32x32x16_bf16 v[16:31], v[48:51], v[68:71], 0\n" \
  "ds_read_b128 v[48:51], v73 offset:52224\n" \
  "s_mov_b64 exec, %[vm]\n" \
  "ds_read_b128 v[52:55], v75 offset:18432\n" \
  "ds_read_b128 v[56:59], v75 offset:18944\n" \
  "s_mov_b64 exec, -1\n" \
  "buffer_load_dwordx2 v[222:223], v123, %[rx], %[sxr] offen offset:16\n" \
  "v_exp_f32 v38, v38\n" \
  "v_exp_f32 v39, v39\n" \
  "v_exp_f32 v40, v40\n" \
  "v_exp_f32 v41, v41\n" \
  "v_smfmac_f32_32x32x32_bf16 v[0:15], v[60:63], v[32:39], v72\n" \
  "v_exp_f32 v42, v42\n" \
  "v_exp_f32 v43, v43\n" \
  "v_exp_f32 v44, v44\n" \
  "v_exp_f32 v45, v45\n" \
  "v_exp_f32 v46, v46\n" \
  "v_exp_f32 v47, v47\n" \
  "v_exp_f32 v16, v16\n" \
  "v_exp_f32 v17, v17\n" \
  "v_smfmac_f32_32x32x32_bf16 v[0:15], v[64:67], v[40:47], v72\n" \
  "v_exp_f32 v18, v18\n" \
  "v_exp_f32 v19, v19\n" \
  "v_exp_f32 v20, v20\n" \
  "v_exp_f32 v21, v21\n" \
  "s_waitcnt lgkmcnt(0)\n" \
  "v_mfma_f32_32x32x16_bf16 v[32:47], v[48:51], v[68:71], 0\n" \
  "ds_read_b128 v[48:51], v73 offset:53248\n" \
  "s_mov_b64 exec, %[vm]\n" \
  "ds_read_b128 v[60:63], v75 offset:19456\n" \
  "ds_read_b128 v[64:67], v75 offset:19968\n" \
  "s_mov_b64 exec, -1\n" \
  "buffer_load_dwordx2 v[224:225], v124, %[rwr], 0 offen\n" \
  "v_exp_f32 v22, v22\n" \
  "v_exp_f32 v23, v23\n" \
  "v_exp_f32 v24, v24\n" \
  "v_exp_f32 v25, v25\n" \
  "v_smfmac_f32_32x32x32_bf16 v[0:15], v[52:55], v[16:23], v72\n" \
  "v_exp_f32 v26, v26\n" \
  "v_exp_f32 v27, v27\n" \
  "v_exp_f32 v28, v28\n" \
  "v_exp_f32 v29, v29\n" \
  "v_exp_f32 v30, v30\n" \
  "v_exp_f32 v31, v31\n" \
  "v_exp_f32 v32, v32\n" \
  "v_exp_f32 v33, v33\n" \
  "v_smfmac_f32_32x32x32_bf16 v[0:15], v[56:59], v[24:31], v72\n" \
  "v_exp_f32 v34, v34\n" \
  "v_exp_f32 v35, v35\n" \
  "v_exp_f32 v36, v36\n" \
  "v_exp_f32 v37, v37\n" \
  "s_waitcnt lgkmcnt(0)\n" \
  "v_mfma_f32_32x32x16_bf16 v[16:31], v[48:51], v[68:71], 0\n" \
  "ds_read_b128 v[48:51], v73 offset:54272\n" \
  "s_mov_b64 exec, %[vm]\n" \
  "ds_read_b128 v[52:55], v75 offset:20480\n" \
  "ds_read_b128 v[56:59], v75 offset:20992\n" \
  "s_mov_b64 exec, -1\n" \
  "buffer_load_dwordx4 v[212:215], v98, %[refc], %[sef] offen\n" \
  "s_add_u32 %[sef], %[sef], %[sefs]\n" \
  "v_exp_f32 v38, v38\n" \
  "v_exp_f32 v39, v39\n" \
  "v_exp_f32 v40, v40\n" \
  "v_exp_f32 v41, v41\n" \
  "v_smfmac_f32_32x32x32_bf16 v[0:15], v[60:63], v[32:39], v72\n" \
  "v_exp_f32 v42, v42\n" \
  "v_exp_f32 v43, v43\n" \
  "v_exp_f32 v44, v44\n" \
  "v_exp_f32 v45, v45\n" \
  "v_exp_f32 v46, v46\n" \
  "v_exp_f32 v47, v47\n" \
  "s_setprio 0\n" \
  "v_exp_f32 v16, v16\n" \
  "v_exp_f32 v17, v17\n" \
  "v_smfmac_f32_32x32x32_bf16 v[0:15], v[64:67], v[40:47], v72\n" \
  "v_exp_f32 v18, v18\n" \
  "v_exp_f32 v19, v19\n" \
  "v_exp_f32 v20, v20\n" \
  "v_exp_f32 v21, v21\n" \
  "s_waitcnt lgkmcnt(0)\n" \
  "v_mfma_f32_32x32x16_bf16 v[32:47], v[48:51], v[68:71], 0\n" \
  "ds_read_b128 v[48:51], v73 offset:55296\n" \
  "s_mov_b64 exec, %[vm]\n" \
  "ds_read_b128 v[60:63], v75 offset:21504\n" \
  "ds_read_b128 v[64:67], v75 offset:22016\n" \
  "s_mov_b64 exec, -1\n" \
  "buffer_load_dwordx2 v[226:227], v124, %[rwr], 0 offen offset:16\n" \
  "v_exp_f32 v22, v22\n" \
  "v_exp_f32 v23, v23\n" \
  "v_exp_f32 v24, v24\n" \
  "v_exp_f32 v25, v25\n" \
  "v_smfmac_f32_32x32x32_bf16 v[0:15], v[52:55], v[16:23], v72\n" \
  "v_exp_f32 v26, v26\n" \
  "v_exp_f32 v27, v27\n" \
  "v_exp_f32 v28, v28\n" \
  "v_exp_f32 v29, v29\n" \
  "v_exp_f32 v30, v30\n" \
  "v_exp_f32 v31, v31\n" \
  "v_exp_f32 v32, v32\n" \
  "v_exp_f32 v33, v33\n" \
  "v_smfmac_f32_32x32x32_bf16 v[0:15], v[56:59], v[24:31], v72\n" \
  "v_exp_f32 v34, v34\n" \
  "v_exp_f32 v35, v35\n" \
  "v_exp_f32 v36, v36\n" \
  "v_exp_f32 v37, v37\n" \
  "s_waitcnt lgkmcnt(0)\n" \
  "v_mfma_f32_32x32x16_bf16 v[16:31], v[48:51], v[68:71], 0\n" \
  "ds_read_b128 v[48:51], v73 offset:56320\n" \
  "s_mov_b64 exec, %[vm]\n" \
  "ds_read_b128 v[52:55], v75 offset:22528\n" \
  "ds_read_b128 v[56:59], v75 offset:23040\n" \
  "s_mov_b64 exec, -1\n" \
  "s_barrier\n" \
  "v_exp_f32 v38, v38\n" \
  "v_exp_f32 v39, v39\n" \
  "v_exp_f32 v40, v40\n" \
  "v_exp_f32 v41, v41\n" \
  "v_smfmac_f32_32x32x32_bf16 v[0:15], v[60:63], v[32:39], v72\n" \
  "v_exp_f32 v42, v42\n" \
  "v_exp_f32 v43, v43\n" \
  "v_exp_f32 v44, v44\n" \
  "v_exp_f32 v45, v45\n" \
  "v_exp_f32 v46, v46\n" \
  "v_exp_f32 v47, v47\n" \
  "v_exp_f32 v16, v16\n" \
  "v_exp_f32 v17, v17\n" \
  "v_smfmac_f32_32x32x32_bf16 v[0:15], v[64:67], v[40:47], v72\n" \
  "v_exp_f32 v18, v18\n" \
  "v_exp_f32 v19, v19\n" \
  "v_exp_f32 v20, v20\n" \
  "v_exp_f32 v21, v21\n" \
  "s_waitcnt lgkmcnt(0)\n" \
  "v_mfma_f32_32x32x16_bf16 v[32:47], v[48:51], v[68:71], 0\n" \
  "ds_read_b128 v[48:51], v73 offset:57344\n" \
  "s_mov_b64 exec, %[vm]\n" \
  "ds_read_b128 v[60:63], v75 offset:23552\n" \
  "ds_read_b128 v[64:67], v75 offset:24064\n" \
  "s_mov_b64 exec, -1\n" \
  "buffer_load_dwordx4 v[228:231], v94, %[rwr], 0 offen\n" \
  "buffer_load_dwordx4 v[232:235], v94, %[ws], %[spf] offen sc1\n" \
  "v_exp_f32 v22, v22\n" \
  "v_exp_f32 v23, v23\n" \
  "v_exp_f32 v24, v24\n" \
  "v_exp_f32 v25, v25\n" \
  "v_smfmac_f32_32x32x32_bf16 v[0:15], v[52:55], v[16:23], v72\n" \
  "v_exp_f32 v26, v26\n" \
  "v_exp_f32 v27, v27\n" \
  "v_exp_f32 v28, v28\n" \
  "v_exp_f32 v29, v29\n" \
  "v_exp_f32 v30, v30\n" \
  "v_exp_f32 v31, v31\n" \
  "v_exp_f32 v32, v32\n" \
  "v_exp_f32 v33, v33\n" \
  "v_smfmac_f32_32x32x32_bf16 v[0:15], v[56:59], v[24:31], v72\n" \
  "v_exp_f32 v34, v34\n" \
  "v_exp_f32 v35, v35\n" \
  "v_exp_f32 v36, v36\n" \
  "v_exp_f32 v37, v37\n" \
  "s_waitcnt lgkmcnt(0)\n" \
  "v_mfma_f32_32x32x16_bf16 v[16:31], v[48:51], v[68:71], 0\n" \
  "ds_read_b128 v[48:51], v73 offset:58368\n" \
  "s_mov_b64 exec, %[vm]\n" \
  "ds_read_b128 v[52:55], v75 offset:24576\n" \
  "ds_read_b128 v[56:59], v75 offset:25088\n" \
  "s_mov_b64 exec, -1\n" \
  "buffer_load_dwordx4 v[236:239], v94, %[ws], %[spf] offen offset:1024 sc1\n" \
  "v_exp_f32 v38, v38\n" \
  "v_exp_f32 v39, v39\n" \
  "v_exp_f32 v40, v40\n" \
  "v_exp_f32 v41, v41\n" \
  "v_smfmac_f32_32x32x32_bf16 v[0:15], v[60:63], v[32:39], v72\n" \
  "v_exp_f32 v42, v42\n" \
  "v_exp_f32 v43, v43\n" \
  "v_exp_f32 v44, v44\n" \
  "v_exp_f32 v45, v45\n" \
  "v_exp_f32 v46, v46\n" \
  "v_exp_f32 v47, v47\n" \
  "s_setprio 3\n" \
  "v_exp_f32 v16, v16\n" \
  "v_exp_f32 v17, v17\n" \
  "v_smfmac_f32_32x32x32_bf16 v[0:15], v[64:67], v[40:47], v72\n" \
  "v_exp_f32 v18, v18\n" \
  "v_exp_f32 v19, v19\n" \
  "v_exp_f32 v20, v20\n" \
  "v_exp_f32 v21, v21\n" \
  "s_waitcnt lgkmcnt(0)\n" \
  "v_mfma_f32_32x32x16_bf16 v[32:47], v[48:51], v[68:71], 0\n" \
  "ds_read_b128 v[48:51], v73 offset:59392\n" \
  "s_mov_b64 exec, %[vm]\n" \
  "ds_read_b128 v[60:63], v75 offset:25600\n" \
  "ds_read_b128 v[64:67], v75 offset:26112\n" \
  "s_mov_b64 exec, -1\n" \
  "v_exp_f32 v22, v22\n" \
  "v_exp_f32 v23, v23\n" \
  "v_exp_f32 v24, v24\n" \
  "v_exp_f32 v25, v25\n" \
  "v_smfmac_f32_32x32x32_bf16 v[0:15], v[52:55], v[16:23], v72\n" \
  "v_exp_f32 v26, v26\n" \
  "v_exp_f32 v27, v27\n" \
  "v_exp_f32 v28, v28\n" \
  "v_exp_f32 v29, v29\n" \
  "v_exp_f32 v30, v30\n" \
  "v_exp_f32 v31, v31\n" \
  "v_exp_f32 v32, v32\n" \
  "v_exp_f32 v33, v33\n" \
  "v_smfmac_f32_32x32x32_bf16 v[0:15], v[56:59], v[24:31], v72\n" \
  "v_exp_f32 v34, v34\n" \
  "v_exp_f32 v35, v35\n" \
  "v_exp_f32 v36, v36\n" \
  "v_exp_f32 v37, v37\n" \
  "s_waitcnt lgkmcnt(0)\n" \
  "v_mfma_f32_32x32x16_bf16 v[16:31], v[48:51], v[68:71], 0\n" \
  "ds_read_b128 v[48:51], v73 offset:60416\n" \
  "s_mov_b64 exec, %[vm]\n" \
  "ds_read_b128 v[52:55], v75 offset:26624\n" \
  "ds_read_b128 v[56:59], v75 offset:27136\n" \
  "s_mov_b64 exec, -1\n" \
  "v_exp_f32 v38, v38\n" \
  "v_exp_f32 v39, v39\n" \
  "v_exp_f32 v40, v40\n" \
  "v_exp_f32 v41, v41\n" \
  "v_smfmac_f32_32x32x32_bf16 v[0:15], v[60:63], v[32:39], v72\n" \
  "v_exp_f32 v42, v42\n" \
  "v_exp_f32 v43, v43\n" \
  "v_exp_f32 v44, v44\n" \
  "v_exp_f32 v45, v45\n" \
  "v_exp_f32 v46, v46\n" \
  "v_exp_f32 v47, v47\n" \
  "v_exp_f32 v16, v16\n" \
  "v_exp_f32 v17, v17\n" \
  "v_smfmac_f32_32x32x32_bf16 v[0:15], v[64:67], v[40:47], v72\n" \
  "v_exp_f32 v18, v18\n" \
  "v_exp_f32 v19, v19\n" \
  "v_exp_f32 v20, v20\n" \
  "v_exp_f32 v21, v21\n" \
  "s_waitcnt lgkmcnt(0)\n" \
  "v_mfma_f32_32x32x16_bf16 v[32:47], v[48:51], v[68:71], 0\n" \
  "ds_read_b128 v[48:51], v73 offset:61440\n" \
  "s_mov_b64 exec, %[vm]\n" \
  "ds_read_b128 v[60:63], v75 offset:27648\n" \
  "ds_read_b128 v[64:67], v75 offset:28160\n" \
  "s_mov_b64 exec, -1\n" \
  "v_exp_f32 v22, v22\n" \
  "v_exp_f32 v23, v23\n" \
  "v_exp_f32 v24, v24\n" \
  "v_exp_f32 v25, v25\n" \
  "v_smfmac_f32_32x32x32_bf16 v[0:15], v[52:55], v[16:23], v72\n" \
  "v_exp_f32 v26, v26\n" \
  "v_exp_f32 v27, v27\n" \
  "v_exp_f32 v28, v28\n" \
  "v_exp_f32 v29, v29\n" \
  "v_exp_f32 v30, v30\n" \
  "v_exp_f32 v31, v31\n" \
  "v_exp_f32 v32, v32\n" \
  "v_exp_f32 v33, v33\n" \
  "v_smfmac_f32_32x32x32_bf16 v[0:15], v[56:59], v[24:31], v72\n" \
  "v_exp_f32 v34, v34\n" \
  "v_exp_f32 v35, v35\n" \
  "v_exp_f32 v36, v36\n" \
  "v_exp_f32 v37, v37\n" \
  "s_waitcnt lgkmcnt(0)\n" \
  "v_mfma_f32_32x32x16_bf16 v[16:31], v[48:51], v[68:71], 0\n" \
  "ds_read_b128 v[48:51], v73 offset:62464\n" \
  "s_mov_b64 exec, %[vm]\n" \
  "ds_read_b128 v[52:55], v75 offset:28672\n" \
  "ds_read_b128 v[56:59], v75 offset:29184\n" \
  "s_mov_b64 exec, -1\n" \
  "buffer_load_dwordx4 v[216:219], v98, %[refc], %[sef] offen\n" \
  "s_add_u32 %[sef], %[sef], %[sefs]\n" \
  "v_exp_f32 v38, v38\n" \
  "v_exp_f32 v39, v39\n" \
  "v_exp_f32 v40, v40\n" \
  "v_exp_f32 v41, v41\n" \
  "v_smfmac_f32_32x32x32_bf16 v[0:15], v[60:63], v[32:39], v72\n" \
  "v_exp_f32 v42, v42\n" \
  "v_exp_f32 v43, v43\n" \
  "v_exp_f32 v44, v44\n" \
  "v_exp_f32 v45, v45\n" \
  "v_exp_f32 v46, v46\n" \
  "v_exp_f32 v47, v47\n" \
  "s_setprio 2\n" \
  "v_exp_f32 v16, v16\n" \
  "v_exp_f32 v17, v17\n" \
  "v_smfmac_f32_32x32x32_bf16 v[0:15], v[64:67], v[40:47], v72\n" \
  "v_exp_f32 v18, v18\n" \
  "v_exp_f32 v19, v19\n" \
  "v_exp_f32 v20, v20\n" \
  "v_exp_f32 v21, v21\n" \
  "s_waitcnt lgkmcnt(0)\n" \
  "v_mfma_f32_32x32x16_bf16 v[32:47], v[48:51], v[68:71], 0\n" \
  "ds_read_b128 v[48:51], v73 offset:63488\n" \
  "s_mov_b64 exec, %[vm]\n" \
  "ds_read_b128 v[60:63], v75 offset:29696\n" \
  "ds_read_b128 v[64:67], v75 offset:30208\n" \
  "s_mov_b64 exec, -1\n" \
  "v_exp_f32 v22, v22\n" \
  "v_exp_f32 v23, v23\n" \
  "v_exp_f32 v24, v24\n" \
  "v_exp_f32 v25, v25\n" \
  "v_smfmac_f32_32x32x32_bf16 v[0:15], v[52:55], v[16:23], v72\n" \
  "v_exp_f32 v26, v26\n" \
  "v_exp_f32 v27, v27\n" \
  "v_exp_f32 v28, v28\n" \
  "v_exp_f32 v29, v29\n" \
  "v_exp_f32 v30, v30\n" \
  "v_exp_f32 v31, v31\n" \
  "v_exp_f32 v32, v32\n" \
  "v_exp_f32 v33, v33\n" \
  "v_smfmac_f32_32x32x32_bf16 v[0:15], v[56:59], v[24:31], v72\n" \
  "v_exp_f32 v34, v34\n" \
  "v_exp_f32 v35, v35\n" \
  "v_exp_f32 v36, v36\n" \
  "v_exp_f32 v37, v37\n" \
  "s_waitcnt lgkmcnt(0)\n" \
  "v_mfma_f32_32x32x16_bf16 v[16:31], v[48:51], v[68:71], 0\n" \
  "ds_read_b128 v[48:51], v73 offset:64512\n" \
  "s_mov_b64 exec, %[vm]\n" \
  "ds_read_b128 v[52:55], v75 offset:30720\n" \
  "ds_read_b128 v[56:59], v75 offset:31232\n" \
  "s_mov_b64 exec, -1\n" \
  "v_exp_f32 v38, v38\n" \
  "v_exp_f32 v39, v39\n" \
  "v_exp_f32 v40, v40\n" \
  "v_exp_f32 v41, v41\n" \
  "v_smfmac_f32_32x32x32_bf16 v[0:15], v[60:63], v[32:39], v72\n" \
  "v_exp_f32 v42, v42\n" \
  "v_exp_f32 v43, v43\n" \
  "v_exp_f32 v44, v44\n" \
  "v_exp_f32 v45, v45\n" \
  "v_exp_f32 v46, v46\n" \
  "v_exp_f32 v47, v47\n" \
  "v_exp_f32 v16, v16\n" \
  "v_exp_f32 v17, v17\n" \
  "v_smfmac_f32_32x32x32_bf16 v[0:15], v[64:67], v[40:47], v72\n" \
  "v_exp_f32 v18, v18\n" \
  "v_exp_f32 v19, v19\n" \
  "v_exp_f32 v20, v20\n" \
  "v_exp_f32 v21, v21\n" \
  "s_waitcnt lgkmcnt(0)\n" \
  "v_mfma_f32_32x32x16_bf16 v[32:47], v[48:51], v[68:71], 0\n" \
  "s_mov_b64 exec, %[vm]\n" \
  "ds_read_b128 v[60:63], v75 offset:31744\n" \
  "ds_read_b128 v[64:67], v75 offset:32256\n" \
  "s_mov_b64 exec, -1\n" \
  "v_exp_f32 v22, v22\n" \
  "v_exp_f32 v23, v23\n" \
  "v_exp_f32 v24, v24\n" \
  "v_exp_f32 v25, v25\n" \
  "v_smfmac_f32_32x32x32_bf16 v[0:15], v[52:55], v[16:23], v72\n" \
  "v_exp_f32 v26, v26\n" \
  "v_exp_f32 v27, v27\n" \
  "v_exp_f32 v28, v28\n" \
  "v_exp_f32 v29, v29\n" \
  "v_exp_f32 v30, v30\n" \
  "v_exp_f32 v31, v31\n" \
  "v_exp_f32 v32, v32\n" \
  "v_exp_f32 v33, v33\n" \
  "v_smfmac_f32_32x32x32_bf16 v[0:15], v[56:59], v[24:31], v72\n" \
  "v_exp_f32 v34, v34\n" \
  "v_exp_f32 v35, v35\n" \
  "v_exp_f32 v36, v36\n" \
  "v_exp_f32 v37, v37\n" \
  "s_waitcnt lgkmcnt(0)\n" \
  "v_exp_f32 v38, v38\n" \
  "v_exp_f32 v39, v39\n" \
  "v_exp_f32 v40, v40\n" \
  "v_exp_f32 v41, v41\n" \
  "v_smfmac_f32_32x32x32_bf16 v[0:15], v[60:63], v[32:39], v72\n" \
  "v_exp_f32 v42, v42\n" \
  "v_exp_f32 v43, v43\n" \
  "v_exp_f32 v44, v44\n" \
  "v_exp_f32 v45, v45\n" \
  "v_exp_f32 v46, v46\n" \
  "v_exp_f32 v47, v47\n" \
  "s_nop 1\n" \
  "v_smfmac_f32_32x32x32_bf16 v[0:15], v[64:67], v[40:47], v72\n" \
  "s_nop 7\n" \
  "s_nop 7\n" \
  "s_setprio 0\n" \
  "s_waitcnt vmcnt(0)\n" \
  "s_branch 20f\n" \
  "1011:\n" \
  "s_mov_b32 %[sc], 0\n" \
  "3011:\n" \
  "buffer_load_dwordx4 v[100:103], v94, %[ws], %[k2] offen sc1\n" \
  "buffer_load_dwordx4 v[104:107], v94, %[wv2], %[k2] offen sc1\n" \
  "s_sleep 1\n" \
  "s_add_u32 %[sc], %[sc], 1\n" \
  "s_waitcnt vmcnt(0)\n" \
  "v_max3_u32 v92, v100, v101, v102\n" \
  "v_max3_u32 v92, v92, v103, v104\n" \
  "v_max3_u32 v92, v92, v105, v106\n" \
  "v_max_u32 v92, v92, v107\n" \
  "v_cmp_eq_u32 vcc, -1, v92\n" \
  "s_cmp_lt_u32 %[sc], 0x20000\n" \
  "s_cselect_b64 %[sp], -1, 0\n" \
  "s_and_b64 vcc, vcc, %[sp]\n" \
  "s_cbranch_vccnz 3011b\n" \
  "s_branch 11b\n" \
  "1012:\n" \
  "ds_read_b32 v93, v89 offset:8\n" \
  "s_waitcnt lgkmcnt(0)\n" \
  "v_readfirstlane_b32 %[sc], v93\n" \
  "s_nop 0\n" \
  "s_cmp_lt_u32 %[sc], 8\n" \
  "s_cbranch_scc1 1012b\n" \
  "s_branch 12b\n" \
  "1013:\n" \
  "s_mov_b32 %[sc], 0\n" \
  "3013:\n" \
  "buffer_load_dwordx4 v[108:111], v94, %[ws], %[k3] offen sc1\n" \
  "buffer_load_dwordx4 v[112:115], v94, %[wv2], %[k3] offen sc1\n" \
  "s_sleep 1\n" \
  "s_add_u32 %[sc], %[sc], 1\n" \
  "s_waitcnt vmcnt(0)\n" \
  "v_max3_u32 v92, v108, v109, v110\n" \
  "v_max3_u32 v92, v92, v111, v112\n" \
  "v_max3_u32 v92, v92, v113, v114\n" \
  "v_max_u32 v92, v92, v115\n" \
  "v_cmp_eq_u32 vcc, -1, v92\n" \
  "s_cmp_lt_u32 %[sc], 0x20000\n" \
  "s_cselect_b64 %[sp], -1, 0\n" \
  "s_and_b64 vcc, vcc, %[sp]\n" \
  "s_cbranch_vccnz 3013b\n" \
  "s_branch 13b\n" \
  "1014:\n" \
  "s_mov_b32 %[sc], 0\n" \
  "3014:\n" \
  "buffer_load_dwordx4 v[100:103], v94, %[ws], %[k4] offen sc1\n" \
  "buffer_load_dwordx4 v[104:107], v94, %[wv2], %[k4] offen sc1\n" \
  "s_sleep 1\n" \
  "s_add_u32 %[sc], %[sc], 1\n" \
  "s_waitcnt vmcnt(0)\n" \
  "v_max3_u32 v92, v100, v101, v102\n" \
  "v_max3_u32 v92, v92, v103, v104\n" \
  "v_max3_u32 v92, v92, v105, v106\n" \
  "v_max_u32 v92, v92, v107\n" \
  "v_cmp_eq_u32 vcc, -1, v92\n" \
  "s_cmp_lt_u32 %[sc], 0x20000\n" \
  "s_cselect_b64 %[sp], -1, 0\n" \
  "s_and_b64 vcc, vcc, %[sp]\n" \
  "s_cbranch_vccnz 3014b\n" \
  "s_branch 14b\n" \
  "1015:\n" \
  "ds_read_b32 v93, v89 offset:16\n" \
  "s_waitcnt lgkmcnt(0)\n" \
  "v_readfirstlane_b32 %[sc], v93\n" \
  "s_nop 0\n" \
  "s_cmp_lt_u32 %[sc], 8\n" \
  "s_cbranch_scc1 1015b\n" \
  "s_branch 15b\n" \
  "1016:\n" \
  "s_mov_b32 %[sc], 0\n" \
  "3016:\n" \
  "buffer_load_dwordx4 v[108:111], v94, %[ws], %[k5] offen sc1\n" \
  "buffer_load_dwordx4 v[112:115], v94, %[wv2], %[k5] offen sc1\n" \
  "s_sleep 1\n" \
  "s_add_u32 %[sc], %[sc], 1\n" \
  "s_waitcnt vmcnt(0)\n" \
  "v_max3_u32 v92, v108, v109, v110\n" \
  "v_max3_u32 v92, v92, v111, v112\n" \
  "v_max3_u32 v92, v92, v113, v114\n" \
  "v_max_u32 v92, v92, v115\n" \
  "v_cmp_eq_u32 vcc, -1, v92\n" \
  "s_cmp_lt_u32 %[sc], 0x20000\n" \
  "s_cselect_b64 %[sp], -1, 0\n" \
  "s_and_b64 vcc, vcc, %[sp]\n" \
  "s_cbranch_vccnz 3016b\n" \
  "s_branch 16b\n" \
  "81:\n" \
  "s_mov_b64 exec, 1\n" \
  "buffer_store_dwordx2 v[78:79], off, %[ws], %[sfo] sc1\n" \
  "s_mov_b64 exec, -1\n" \
  "s_branch 80b\n" \
  "1017:\n" \
  "s_mov_b32 %[sc], 0\n" \
  "3017:\n" \
  "buffer_load_dwordx4 v[100:103], v94, %[ws], %[k6] offen sc1\n" \
  "buffer_load_dwordx4 v[104:107], v94, %[wv2], %[k6] offen sc1\n" \
  "s_sleep 1\n" \
  "s_add_u32 %[sc], %[sc], 1\n" \
  "s_waitcnt vmcnt(0)\n" \
  "v_max3_u32 v92, v100, v101, v102\n" \
  "v_max3_u32 v92, v92, v103, v104\n" \
  "v_max3_u32 v92, v92, v105, v106\n" \
  "v_max_u32 v92, v92, v107\n" \
  "v_cmp_eq_u32 vcc, -1, v92\n" \
  "s_cmp_lt_u32 %[sc], 0x20000\n" \
  "s_cselect_b64 %[sp], -1, 0\n" \
  "s_and_b64 vcc, vcc, %[sp]\n" \
  "s_cbranch_vccnz 3017b\n" \
  "s_branch 17b\n" \
  "1018:\n" \
  "ds_read_b32 v93, v89 offset:24\n" \
  "s_waitcnt lgkmcnt(0)\n" \
  "v_readfirstlane_b32 %[sc], v93\n" \
  "s_nop 0\n" \
  "s_cmp_lt_u32 %[sc], 8\n" \
  "s_cbranch_scc1 1018b\n" \
  "s_branch 18b\n" \
  "1019:\n" \
  "s_mov_b32 %[sc], 0\n" \
  "3019:\n" \
  "buffer_load_dwordx4 v[108:111], v94, %[ws], %[k7] offen sc1\n" \
  "buffer_load_dwordx4 v[112:115], v94, %[wv2], %[k7] offen sc1\n" \
  "s_sleep 1\n" \
  "s_add_u32 %[sc], %[sc], 1\n" \
  "s_waitcnt vmcnt(0)\n" \
  "v_max3_u32 v92, v108, v109, v110\n" \
  "v_max3_u32 v92, v92, v111, v112\n" \
  "v_max3_u32 v92, v92, v113, v114\n" \
  "v_max_u32 v92, v92, v115\n" \
  "v_cmp_eq_u32 vcc, -1, v92\n" \
  "s_cmp_lt_u32 %[sc], 0x20000\n" \
  "s_cselect_b64 %[sp], -1, 0\n" \
  "s_and_b64 vcc, vcc, %[sp]\n" \
  "s_cbranch_vccnz 3019b\n" \
  "s_branch 19b\n" \
  "20:\n"
#define ATTN_F \
  "ds_read_b128 v[48:51], v73 offset:0\n" \
  "v_mov_b32 v52, v91\n" \
  "v_mov_b32 v53, v91\n" \
  "v_mov_b32 v54, v91\n" \
  "v_mov_b32 v55, v91\n" \
  "v_mov_b32 v56, v91\n" \
  "v_mov_b32 v57, v91\n" \
  "v_mov_b32 v58, v91\n" \
  "v_mov_b32 v59, v91\n" \
  "v_mov_b32 v60, v91\n" \
  "v_mov_b32 v61, v91\n" \
  "v_mov_b32 v62, v91\n" \
  "v_mov_b32 v63, v91\n" \
  "v_mov_b32 v64, v91\n" \
  "v_mov_b32 v65, v91\n" \
  "v_mov_b32 v66, v91\n" \
  "v_mov_b32 v67, v91\n" \
  "s_mov_b64 exec, %[vm]\n" \
  "ds_read_b128 v[52:55], v74 offset:0\n" \
  "ds_read_b128 v[56:59], v74 offset:512\n" \
  "s_mov_b64 exec, -1\n" \
  "v_mov_b32 v0, 0\n" \
  "v_mov_b32 v1, 0\n" \
  "v_mov_b32 v2, 0\n" \
  "v_mov_b32 v3, 0\n" \
  "v_mov_b32 v4, 0\n" \
  "v_mov_b32 v5, 0\n" \
  "v_mov_b32 v6, 0\n" \
  "v_mov_b32 v7, 0\n" \
  "v_mov_b32 v8, 0\n" \
  "v_mov_b32 v9, 0\n" \
  "v_mov_b32 v10, 0\n" \
  "v_mov_b32 v11, 0\n" \
  "v_mov_b32 v12, 0\n" \
  "v_mov_b32 v13, 0\n" \
  "v_mov_b32 v14, 0\n" \
  "v_mov_b32 v15, 0\n" \
  "s_waitcnt lgkmcnt(2)\n" \
  "v_mfma_f32_32x32x16_bf16 v[16:31], v[48:51], v[68:71], v[100:115]\n" \
  "ds_read_b128 v[48:51], v73 offset:1024\n" \
  "v_mov_b32 v40, 0\n" \
  "v_mov_b32 v41, 0\n" \
  "v_mov_b32 v42, 0\n" \
  "v_mov_b32 v43, 0\n" \
  "v_mov_b32 v44, 0\n" \
  "v_mov_b32 v45, 0\n" \
  "v_mov_b32 v46, 0\n" \
  "v_mov_b32 v47, 0\n" \
  "s_nop 1\n" \
  "1:\n" \
  "v_exp_f32 v16, v16\n" \
  "v_exp_f32 v17, v17\n" \
  "v_smfmac_f32_32x32x32_bf16 v[0:15], v[64:67], v[40:47], v72\n" \
  "v_exp_f32 v18, v18\n" \
  "v_exp_f32 v19, v19\n" \
  "v_exp_f32 v20, v20\n" \
  "v_exp_f32 v21, v21\n" \
  "s_waitcnt lgkmcnt(0)\n" \
  "v_mfma_f32_32x32x16_bf16 v[32:47], v[48:51], v[68:71], v[100:115]\n" \
  "ds_read_b128 v[48:51], v73 offset:2048\n" \
  "s_mov_b64 exec, %[vm]\n" \
  "ds_read_b128 v[60:63], v74 offset:1024\n" \
  "ds_read_b128 v[64:67], v74 offset:1536\n" \
  "s_mov_b64 exec, -1\n" \
  "v_exp_f32 v22, v22\n" \
  "v_exp_f32 v23, v23\n" \
  "v_exp_f32 v24, v24\n" \
  "v_exp_f32 v25, v25\n" \
  "v_smfmac_f32_32x32x32_bf16 v[0:15], v[52:55], v[16:23], v72\n" \
  "v_exp_f32 v26, v26\n" \
  "v_exp_f32 v27, v27\n" \
  "v_exp_f32 v28, v28\n" \
  "v_exp_f32 v29, v29\n" \
  "v_exp_f32 v30, v30\n" \
  "v_exp_f32 v31, v31\n" \
  "v_exp_f32 v32, v32\n" \
  "v_exp_f32 v33, v33\n" \
  "v_smfmac_f32_32x32x32_bf16 v[0:15], v[56:59], v[24:31], v72\n" \
  "v_exp_f32 v34, v34\n" \
  "v_exp_f32 v35, v35\n" \
  "v_exp_f32 v36, v36\n" \
  "v_exp_f32 v37, v37\n" \
  "s_waitcnt lgkmcnt(0)\n" \
  "v_mfma_f32_32x32x16_bf16 v[16:31], v[48:51], v[68:71], v[100:115]\n" \
  "ds_read_b128 v[48:51], v73 offset:3072\n" \
  "s_mov_b64 exec, %[vm]\n" \
  "ds_read_b128 v[52:55], v74 offset:2048\n" \
  "ds_read_b128 v[56:59], v74 offset:2560\n" \
  "s_mov_b64 exec, -1\n" \
  "v_exp_f32 v38, v38\n" \
  "v_exp_f32 v39, v39\n" \
  "v_exp_f32 v40, v40\n" \
  "v_exp_f32 v41, v41\n" \
  "v_smfmac_f32_32x32x32_bf16 v[0:15], v[60:63], v[32:39], v72\n" \
  "v_exp_f32 v42, v42\n" \
  "v_exp_f32 v43, v43\n" \
  "v_exp_f32 v44, v44\n" \
  "v_exp_f32 v45, v45\n" \
  "v_exp_f32 v46, v46\n" \
  "v_exp_f32 v47, v47\n" \
  "v_exp_f32 v16, v16\n" \
  "v_exp_f32 v17, v17\n" \
  "v_smfmac_f32_32x32x32_bf16 v[0:15], v[64:67], v[40:47], v72\n" \
  "v_exp_f32 v18, v18\n" \
  "v_exp_f32 v19, v19\n" \
  "v_exp_f32 v20, v20\n" \
  "v_exp_f32 v21, v21\n" \
  "s_waitcnt lgkmcnt(0)\n" \
  "v_mfma_f32_32x32x16_bf16 v[32:47], v[48:51], v[68:71], v[100:115]\n" \
  "ds_read_b128 v[48:51], v73 offset:4096\n" \
  "s_mov_b64 exec, %[vm]\n" \
  "ds_read_b128 v[60:63], v74 offset:3072\n" \
  "ds_read_b128 v[64:67], v74 offset:3584\n" \
  "s_mov_b64 exec, -1\n" \
  "v_exp_f32 v22, v22\n" \
  "v_exp_f32 v23, v23\n" \
  "v_exp_f32 v24, v24\n" \
  "v_exp_f32 v25, v25\n" \
  "v_smfmac_f32_32x32x32_bf16 v[0:15], v[52:55], v[16:23], v72\n" \
  "v_exp_f32 v26, v26\n" \
  "v_exp_f32 v27, v27\n" \
  "v_exp_f32 v28, v28\n" \
  "v_exp_f32 v29, v29\n" \
  "v_exp_f32 v30, v30\n" \
  "v_exp_f32 v31, v31\n" \
  "v_exp_f32 v32, v32\n" \
  "v_exp_f32 v33, v33\n" \
  "v_smfmac_f32_32x32x32_bf16 v[0:15], v[56:59], v[24:31], v72\n" \
  "v_exp_f32 v34, v34\n" \
  "v_exp_f32 v35, v35\n" \
  "v_exp_f32 v36, v36\n" \
  "v_exp_f32 v37, v37\n" \
  "s_waitcnt lgkmcnt(0)\n" \
  "v_mfma_f32_32x32x16_bf16 v[16:31], v[48:51], v[68:71], v[100:115]\n" \
  "ds_read_b128 v[48:51], v73 offset:5120\n" \
  "s_mov_b64 exec, %[vm]\n" \
  "ds_read_b128 v[52:55], v74 offset:4096\n" \
  "ds_read_b128 v[56:59], v74 offset:4608\n" \
  "s_mov_b64 exec, -1\n" \
  "v_exp_f32 v38, v38\n" \
  "v_exp_f32 v39, v39\n" \
  "v_exp_f32 v40, v40\n" \
  "v_exp_f32 v41, v41\n" \
  "v_smfmac_f32_32x32x32_bf16 v[0:15], v[60:63], v[32:39], v72\n" \
  "v_exp_f32 v42, v42\n" \
  "v_exp_f32 v43, v43\n" \
  "v_exp_f32 v44, v44\n" \
  "v_exp_f32 v45, v45\n" \
  "v_exp_f32 v46, v46\n" \
  "v_exp_f32 v47, v47\n" \
  "v_exp_f32 v16, v16\n" \
  "v_exp_f32 v17, v17\n" \
  "v_smfmac_f32_32x32x32_bf16 v[0:15], v[64:67], v[40:47], v72\n" \
  "v_exp_f32 v18, v18\n" \
  "v_exp_f32 v19, v19\n" \
  "v_exp_f32 v20, v20\n" \
  "v_exp_f32 v21, v21\n" \
  "s_waitcnt lgkmcnt(0)\n" \
  "v_mfma_f32_32x32x16_bf16 v[32:47], v[48:51], v[68:71], v[100:115]\n" \
  "ds_read_b128 v[48:51], v73 offset:6144\n" \
  "s_mov_b64 exec, %[vm]\n" \
  "ds_read_b128 v[60:63], v74 offset:5120\n" \
  "ds_read_b128 v[64:67], v74 offset:5632\n" \
  "s_mov_b64 exec, -1\n" \
  "v_exp_f32 v22, v22\n" \
  "v_exp_f32 v23, v23\n" \
  "v_exp_f32 v24, v24\n" \
  "v_exp_f32 v25, v25\n" \
  "v_smfmac_f32_32x32x32_bf16 v[0:15], v[52:55], v[16:23], v72\n" \
  "v_exp_f32 v26, v26\n" \
  "v_exp_f32 v27, v27\n" \
  "v_exp_f32 v28, v28\n" \
  "v_exp_f32 v29, v29\n" \
  "v_exp_f32 v30, v30\n" \
  "v_exp_f32 v31, v31\n" \
  "v_exp_f32 v32, v32\n" \
  "v_exp_f32 v33, v33\n" \
  "v_smfmac_f32_32x32x32_bf16 v[0:15], v[56:59], v[24:31], v72\n" \
  "v_exp_f32 v34, v34\n" \
  "v_exp_f32 v35, v35\n" \
  "v_exp_f32 v36, v36\n" \
  "v_exp_f32 v37, v37\n" \
  "s_waitcnt lgkmcnt(0)\n" \
  "v_mfma_f32_32x32x16_bf16 v[16:31], v[48:51], v[68:71], v[100:115]\n" \
  "ds_read_b128 v[48:51], v73 offset:7168\n" \
  "s_mov_b64 exec, %[vm]\n" \
  "ds_read_b128 v[52:55], v74 offset:6144\n" \
  "ds_read_b128 v[56:59], v74 offset:6656\n" \
  "s_mov_b64 exec, -1\n" \
  "v_exp_f32 v38, v38\n" \
  "v_exp_f32 v39, v39\n" \
  "v_exp_f32 v40, v40\n" \
  "v_exp_f32 v41, v41\n" \
  "v_smfmac_f32_32x32x32_bf16 v[0:15], v[60:63], v[32:39], v72\n" \
  "v_exp_f32 v42, v42\n" \
  "v_exp_f32 v43, v43\n" \
  "v_exp_f32 v44, v44\n" \
  "v_exp_f32 v45, v45\n" \
  "v_exp_f32 v46, v46\n" \
  "v_exp_f32 v47, v47\n" \
  "v_exp_f32 v16, v16\n" \
  "v_exp_f32 v17, v17\n" \
  "v_smfmac_f32_32x32x32_bf16 v[0:15], v[64:67], v[40:47], v72\n" \
  "v_exp_f32 v18, v18\n" \
  "v_exp_f32 v19, v19\n" \
  "v_exp_f32 v20, v20\n" \
  "v_exp_f32 v21, v21\n" \
  "s_waitcnt lgkmcnt(0)\n" \
  "v_mfma_f32_32x32x16_bf16 v[32:47], v[48:51], v[68:71], v[100:115]\n" \
  "ds_read_b128 v[48:51], v73 offset:8192\n" \
  "s_mov_b64 exec, %[vm]\n" \
  "ds_read_b128 v[60:63], v74 offset:7168\n" \
  "ds_read_b128 v[64:67], v74 offset:7680\n" \
  "s_mov_b64 exec, -1\n" \
  "v_exp_f32 v22, v22\n" \
  "v_exp_f32 v23, v23\n" \
  "v_exp_f32 v24, v24\n" \
  "v_exp_f32 v25, v25\n" \
  "v_smfmac_f32_32x32x32_bf16 v[0:15], v[52:55], v[16:23], v72\n" \
  "v_exp_f32 v26, v26\n" \
  "v_exp_f32 v27, v27\n" \
  "v_exp_f32 v28, v28\n" \
  "v_exp_f32 v29, v29\n" \
  "v_exp_f32 v30, v30\n" \
  "v_exp_f32 v31, v31\n" \
  "v_exp_f32 v32, v32\n" \
  "v_exp_f32 v33, v33\n" \
  "v_smfmac_f32_32x32x32_bf16 v[0:15], v[56:59], v[24:31], v72\n" \
  "v_exp_f32 v34, v34\n" \
  "v_exp_f32 v35, v35\n" \
  "v_exp_f32 v36, v36\n" \
  "v_exp_f32 v37, v37\n" \
  "s_waitcnt lgkmcnt(0)\n" \
  "v_mfma_f32_32x32x16_bf16 v[16:31], v[48:51], v[68:71], v[100:115]\n" \
  "ds_read_b128 v[48:51], v73 offset:9216\n" \
  "s_mov_b64 exec, %[vm]\n" \
  "ds_read_b128 v[52:55], v74 offset:8192\n" \
  "ds_read_b128 v[56:59], v74 offset:8704\n" \
  "s_mov_b64 exec, -1\n" \
  "v_exp_f32 v38, v38\n" \
  "v_exp_f32 v39, v39\n" \
  "v_exp_f32 v40, v40\n" \
  "v_exp_f32 v41, v41\n" \
  "v_smfmac_f32_32x32x32_bf16 v[0:15], v[60:63], v[32:39], v72\n" \
  "v_exp_f32 v42, v42\n" \
  "v_exp_f32 v43, v43\n" \
  "v_exp_f32 v44, v44\n" \
  "v_exp_f32 v45, v45\n" \
  "v_exp_f32 v46, v46\n" \
  "v_exp_f32 v47, v47\n" \
  "v_add_u32 v73, 0x2000, v73\n" \
  "v_add_u32 v74, 8192, v74\n" \
  "s_sub_u32 %[n], %[n], 1\n" \
  "s_cmp_lg_u32 %[n], 0\n" \
  "s_cbranch_scc1 1b\n" \
  "s_nop 1\n" \
  "v_smfmac_f32_32x32x32_bf16 v[0:15], v[64:67], v[40:47], v72\n" \
  "s_waitcnt lgkmcnt(0)\n" \
  "s_nop 7\n" \
  "s_nop 7\n"

constexpr unsigned WS_EPOCH = 0, WS_TMO = 256;
constexpr unsigned WS_MODC = 4096;
constexpr unsigned WS_MODN = WS_MODC + 131072;
constexpr unsigned WS_LNST = WS_MODN + 16384;
constexpr unsigned WS_PREF = WS_LNST + 32768;
constexpr unsigned WS_KVF  = WS_PREF + 2048;
constexpr unsigned WS_MODP = WS_KVF + 32768;
constexpr unsigned WS_KF   = 458752;
constexpr unsigned WS_VF   = WS_KF + (2u << 20);
constexpr unsigned WS_OF   = WS_VF + (2u << 20);
constexpr unsigned WS_ST   = WS_OF + (2u << 20);
constexpr unsigned WS_CAND = WS_ST + 131072;
constexpr unsigned WS_GU   = WS_CAND + 8192;
constexpr unsigned WS_ROWS = WS_GU + 4 * 7168;
constexpr unsigned WS_END  = WS_ROWS + 262144;
constexpr unsigned WS_BYTES = 8u << 20;
static_assert(WS_MODP + 131072 <= WS_KF && WS_END <= WS_BYTES, "ws map");
constexpr unsigned SENT = 0xFFFFFFFFu;
constexpr unsigned VG = 512;
constexpr unsigned L_RED = 0;
constexpr unsigned L_MODW = 2048;
constexpr unsigned L_LN = 4096;
constexpr unsigned L_QS = 6144;
constexpr unsigned L_MODN = 14336;
constexpr unsigned L_TOK = 22528;
constexpr unsigned L_RED2 = 20480;
constexpr unsigned L_SEL = 23040;
constexpr unsigned L_CNT = 22976;
constexpr unsigned L_AUG = 23104;
constexpr unsigned L_PST = 26176;
constexpr unsigned L_ERED = L_PST;
constexpr unsigned L_K = 29312, L_V = L_K + 65536, L_TOTAL = L_V + 130 * VG;
constexpr unsigned L_WP = L_K + 8192;
constexpr unsigned L_WRAW = (L_V + 16 * VG + 127) / 128 * 128;
constexpr unsigned L_OST = L_K;
constexpr unsigned L_XCH = L_K + 32768;
static_assert(L_TOTAL <= 163840 && L_WRAW + 49152 <= L_TOTAL, "lds");

constexpr unsigned SPIN_CAP = 1u << 17;
constexpr float QSCALE = 0.25f * 1.4426950408889634f;

#define STAMP(i) do {} while (0)
#define STAMPC(i) do {} while (0)
#define STAMP_DECL
#define STAMP_FLUSH(n)
#define SB() __builtin_amdgcn_sched_barrier(0)
#define PIN(x) asm volatile("" : "+v"(x))

struct Args {
  const u16* x; const u16* wqkv; const u16* wo; const u16* wr; const u16* wg; const u16* wu; const u16* wd; const u16* c; const u16* wm;
  u16* out; char* ws;
};
typedef __amdgpu_buffer_rsrc_t rsrc_t;
DEV rsrc_t mkrs(const void* p, unsigned bytes) { return __builtin_amdgcn_make_buffer_rsrc((void*)p, 0, bytes, 0x00020000); }
DEV float lo2f(unsigned v) { return __uint_as_float(v << 16); }
DEV float hi2f(unsigned v) { return __uint_as_float(v & 0xffff0000u); }
DEV unsigned cvtpk(float lo, float hi) { const f32x2 f = {lo, hi}; return __builtin_bit_cast(unsigned, __builtin_convertvector(f, bf16x2)); }
DEV float rbf(float f) { return __uint_as_float(cvtpk(f, f) << 16); }
DEV unsigned nosent(unsigned v) { return v < 0xFFFFFFFEu ? v : 0xFFFFFFFEu; }
DEV f32x16 mfma32(v4u a, v4u b, f32x16 c) { return __builtin_amdgcn_mfma_f32_32x32x16_bf16(__builtin_bit_cast(bf16x8, a), __builtin_bit_cast(bf16x8, b), c, 0, 0, 0); }
DEV f32x4 mfma16(v4u a, v4u b, f32x4 c) { return __builtin_amdgcn_mfma_f32_16x16x32_bf16(__builtin_bit_cast(bf16x8, a), __builtin_bit_cast(bf16x8, b), c, 0, 0, 0); }
DEV float dot2(unsigned a, unsigned b, float c) { return __builtin_amdgcn_fdot2_f32_bf16(__builtin_bit_cast(bf16x2, a), __builtin_bit_cast(bf16x2, b), c, false); }
DEV LAS char* lp(unsigned base) { return (LAS char*)(size_t)base; }
#define LR16(base, off) (*(LAS v4u*)(lp(base) + (off)))
#define LR8(base, off) (*(LAS v2u*)(lp(base) + (off)))
#define LRF4(base, off) (*(LAS f32x4*)(lp(base) + (off)))
#define LRF(base, off) (*(LAS float*)(lp(base) + (off)))
DEV v2u lds_tr(unsigned base, unsigned off) { return __builtin_bit_cast(v2u, __builtin_amdgcn_ds_read_tr16_b64_v4i16((LAS s16x4*)(lp(base) + off))); }
DEV void lds_addf(unsigned base, unsigned off, float v) { __hip_atomic_fetch_add((LAS float*)(lp(base) + off), v, __ATOMIC_RELAXED, __HIP_MEMORY_SCOPE_WORKGROUP); }
DEV v4u bld16(rsrc_t r, unsigned v, unsigned s) { return __builtin_amdgcn_raw_buffer_load_b128(r, v, s, 0); }
DEV v2u bld8(rsrc_t r, unsigned v, unsigned s) { return __builtin_amdgcn_raw_buffer_load_b64(r, v, s, 0); }
DEV v2u ld8_sc1(rsrc_t r, unsigned v, unsigned s) { return __builtin_amdgcn_raw_buffer_load_b64(r, v, s, 16); }
DEV v4u ld16_sc1(rsrc_t r, unsigned v, unsigned s) { return __builtin_amdgcn_raw_buffer_load_b128(r, v, s, 16); }
DEV void st4_sc1(rsrc_t r, unsigned v, unsigned s, unsigned x) { __builtin_amdgcn_raw_buffer_store_b32(x, r, v, s, 16); }
DEV void st8_sc1(rsrc_t r, unsigned v, unsigned s, v2u x) { __builtin_amdgcn_raw_buffer_store_b64(x, r, v, s, 16); }
DEV void st16_sc1(rsrc_t r, unsigned v, unsigned s, v4u x) { __builtin_amdgcn_raw_buffer_store_b128(x, r, v, s, 16); }
DEV v2u granule(unsigned t32, unsigned t8, float v) { return v2u{(__float_as_uint(v) & 0xffffff00u) | t8, t32}; }
DEV float gval(unsigned lo) { return __uint_as_float(lo & 0xffffff00u); }
DEV unsigned gran_bad(unsigned lo, unsigned hi, unsigned t32, unsigned t8) { return (hi ^ t32) | ((lo ^ t8) & 0xffu); }
DEV unsigned umax4(v4u v) { const unsigned a = v[0] > v[1] ? v[0] : v[1], b = v[2] > v[3] ? v[2] : v[3]; return a > b ? a : b; }
template <int CTRL> DEV float dpp_add(float v) { return v + __builtin_bit_cast(float, __builtin_amdgcn_update_dpp(0, __builtin_bit_cast(int, v), CTRL, 0xf, 0xf, true)); }
template <int CTRL> DEV unsigned dpp_umax(unsigned v) { const unsigned o = (unsigned)__builtin_amdgcn_update_dpp(0, (int)v, CTRL, 0xf, 0xf, true); return o > v ? o : v; }
DEV float swap16_add(float v) { auto r = __builtin_amdgcn_permlane16_swap(__float_as_uint(v), __float_as_uint(v), false, false); return __uint_as_float(r[0]) + __uint_as_float(r[1]); }
DEV float swap32_add(float v) { auto r = __builtin_amdgcn_permlane32_swap(__float_as_uint(v), __float_as_uint(v), false, false); return __uint_as_float(r[0]) + __uint_as_float(r[1]); }
DEV float swap32_max(float v) { auto r = __builtin_amdgcn_permlane32_swap(__float_as_uint(v), __float_as_uint(v), false, false); return fmaxf(__uint_as_float(r[0]), __uint_as_float(r[1])); }
DEV unsigned swap16_umax(unsigned v) { auto r = __builtin_amdgcn_permlane16_swap(v, v, false, false); return r[0] > r[1] ? r[0] : r[1]; }
DEV unsigned swap32_umax(unsigned v) { auto r = __builtin_amdgcn_permlane32_swap(v, v, false, false); return r[0] > r[1] ? r[0] : r[1]; }
DEV float wave_sum(float v) { v = dpp_add<0xB1>(v); v = dpp_add<0x4E>(v); v = dpp_add<0x141>(v); v = dpp_add<0x140>(v); v = swap16_add(v); return swap32_add(v); }
DEV unsigned half_umax(unsigned v) { v = dpp_umax<0xB1>(v); v = dpp_umax<0x4E>(v); v = dpp_umax<0x141>(v); v = dpp_umax<0x140>(v); return swap16_umax(v); }
DEV float frcp(float x) { return __builtin_amdgcn_rcpf(x); }
DEV float sigm(float x) { return frcp(1.f + __builtin_amdgcn_exp2f(-1.4426950408889634f * x)); }
DEV bool spin_fail(unsigned& spins, rsrc_t ws) {
  __builtin_amdgcn_s_sleep(1);
  if (++spins > SPIN_CAP) { st4_sc1(ws, WS_TMO, 0, 1u); return true; }
  return false;
}
DEV unsigned mkkey(float v, unsigned tok) { const unsigned u = __float_as_uint(v); const unsigned o = u ^ ((unsigned)((int)u >> 31) | 0x80000000u); return nosent((o & 0xfffff800u) | (2047u - tok)); }
DEV float keyval(unsigned k) { const unsigned o = k & 0xfffff800u; return __uint_as_float((o & 0x80000000u) ? (o ^ 0x80000000u) : ~o); }

__global__ __launch_bounds__(512, 2) void mega(Args a) {
  extern __shared__ __attribute__((aligned(16))) char lds_[];
  const int tid = threadIdx.x;
  const unsigned l = tid & 63, l16 = l * 16;
  const unsigned w = __builtin_amdgcn_readfirstlane(tid >> 6);
  const unsigned b = blockIdx.x, xcd = b & 7, rr = b >> 3;
  const unsigned qb = (xcd & 3) * 2 + (rr & 1), h = (xcd >> 2) * 16 + (rr >> 1);
  STAMP_DECL;
  STAMP(0);
  const rsrc_t ws = mkrs(a.ws, WS_BYTES), rx = mkrs(a.x, 2u << 20), rw = mkrs(a.wqkv, 1536u * 1024u), rm = mkrs(a.wm, 3u << 20), rc = mkrs(a.c, 1024);
  const u64 e0 = *(const GAS u64*)(a.ws + WS_EPOCH);
  unsigned lds0 = (unsigned)(size_t)(LAS char*)lds_;
  unsigned v16 = lds0 + l16;
  PIN(v16);
  __builtin_amdgcn_s_setprio(3);
  const v4u xs = bld16(rx, l16, (8 * b + w) * 1024);
  const v4u nw = bld16(rm, (8 * w + (l >> 3)) * 6144 + (l & 7) * 16, (b & 7) * (64 * 6144) + 2048 + (b >> 3) * 128);
  const unsigned nc = __builtin_amdgcn_raw_buffer_load_b16(rc, (8 * w + (l >> 3)) * 2, (b & 7) * 128, 0);
  const bool modp = b < 32;
  v4u mw[4]; v2u mc;
  if (modp) {
    const unsigned vo = (tid >> 3) * (4 * 6144) + (tid & 7) * 16, so = (b & 1) * (256 * 6144) + (b >> 1) * 128;
#pragma unroll
    for (int i = 0; i < 4; i++) mw[i] = bld16(rm, vo, so + i * 6144);
    mc = bld8(rc, (tid >> 3) * 8, (b & 1) * 512);
  }
  SB();
  const unsigned vX = (l >> 3) * 1024 + (l & 7) * 16, sX = (256 * qb + 32 * w) * 1024;
  v4u xl[8][4];
#define XLOAD(p) { _Pragma("unroll") for (int q = 0; q < 4; q++) xl[p][q] = __builtin_amdgcn_raw_buffer_load_b128(rx, vX + (p) * 128, sX + q * 8192, 0); SB(); }
  XLOAD(0); XLOAD(1);
  const u64 ep = e0 + 0x9E3779B97F4A7C15ull;
  const unsigned t32 = (unsigned)(ep >> 8), t8 = (unsigned)ep & 0xffu;
  if (modp) {
    unsigned sp[4];
#pragma unroll
    for (int i = 0; i < 4; i++) { const float cv = (i & 1) ? hi2f(mc[i >> 1]) : lo2f(mc[i >> 1]); sp[i] = cvtpk(cv * sigm(cv), 0.f); }
    float acc[8];
#pragma unroll
    for (int j = 0; j < 4; j++) {
      acc[2 * j] = dot2(mw[0][j], sp[0], 0.f); acc[2 * j + 1] = dot2(mw[0][j], sp[0] << 16, 0.f);
#pragma unroll
      for (int i = 1; i < 4; i++) { acc[2 * j] = dot2(mw[i][j], sp[i], acc[2 * j]); acc[2 * j + 1] = dot2(mw[i][j], sp[i] << 16, acc[2 * j + 1]); }
    }
#pragma unroll
    for (int j = 0; j < 8; j++) acc[j] = swap32_add(swap16_add(dpp_add<0x128>(acc[j])));
    if (l < 8) { LRF4(lds0 + w * 256 + l * 32, L_RED) = f32x4{acc[0], acc[1], acc[2], acc[3]}; LRF4(lds0 + w * 256 + l * 32, L_RED + 16) = f32x4{acc[4], acc[5], acc[6], acc[7]}; }
    __syncthreads();
    {
      float s_ = LRF(lds0 + l * 4, L_RED);
#pragma unroll
      for (int i = 1; i < 8; i++) s_ += LRF(lds0 + l * 4, L_RED + i * 256);
      st8_sc1(ws, l * 8, WS_MODC + w * 16384 + ((b & 1) * 1024 + (b >> 1) * 64) * 8, granule(t32, t8, s_));
    }
  }
  SB();
  XLOAD(2); XLOAD(3);
  v4u xf[2][16];
  const unsigned sto_w = (l >> 3) * 128 + (((l & 7) ^ (l >> 4)) * 16), sto_r = (l & 15) * 128 + (((l >> 4) ^ (((l & 15) >> 1) & 7)) * 16);
  unsigned stw0 = lds0 + L_WP + w * 6144 + sto_w, stw1 = stw0 ^ 64, str0 = lds0 + L_WP + w * 6144 + sto_r, str1 = str0 ^ 64;
  PIN(stw0); PIN(stw1); PIN(str0); PIN(str1);
#define XPIECE(p)                                                                              \
  {                                                                                            \
    _Pragma("unroll") for (int q = 0; q < 4; q++) LR16((q & 1) ? stw1 : stw0, q * 1024) = xl[p][q]; \
    _Pragma("unroll") for (int u = 0; u < 2; u++) { xf[u][2 * (p)] = LR16(str0, u * 2048); xf[u][2 * (p) + 1] = LR16(str1, u * 2048); } \
  }
  __builtin_amdgcn_s_setprio(2);
  v4u wl[3][2];
  const unsigned vW = (l >> 1) * 3072 + (l & 1) * 16, sW = w * (64 * 3072) + h * 32;
#define WRLOAD(m) { _Pragma("unroll") for (int i = 0; i < 2; i++) wl[m][i] = bld16(rw, vW, sW + i * (32 * 3072) + (m) * 1024); SB(); }
  WRLOAD(0);
  {
    float s = dot2(xs[0], 0x3F803F80u, 0.f), q = dot2(xs[0], xs[0], 0.f);
#pragma unroll
    for (int j = 1; j < 4; j++) { s = dot2(xs[j], 0x3F803F80u, s); q = dot2(xs[j], xs[j], q); }
    const float mean = wave_sum(s) * (1.f / 512.f);
    const float var = fmaxf(wave_sum(q) * (1.f / 512.f) - mean * mean, 0.f);
    const float sd = rbf(__builtin_amdgcn_sqrtf(rbf(rbf(var) + 1e-5f)));
    st8_sc1(ws, l < 2 ? l * 8 : 0x80000000u, WS_LNST + (8 * b + w) * 16, granule(t32, t8, l ? sd : rbf(mean)));
  }
  XPIECE(0);
  SB();
  WRLOAD(1);
  XPIECE(1);
  SB();
  WRLOAD(2);
  STAMP(27);
  XPIECE(2);
  SB();
  __builtin_amdgcn_s_setprio(1);
  v2u mg[4];
  const unsigned smg = WS_MODC + xcd * 16384 + w * 512;
#pragma unroll
  for (int i = 0; i < 4; i++) mg[i] = ld8_sc1(ws, l * 8, smg + (i & 1) * 8192 + (i >> 1) * 4096);
  SB();
  XLOAD(4);
  XPIECE(3);
  {
    const float cv = lo2f(nc);
    const unsigned sp = cvtpk(cv * sigm(cv), 0.f);
    float v[8];
#pragma unroll
    for (int j = 0; j < 4; j++) { v[2 * j] = dot2(nw[j], sp, 0.f); v[2 * j + 1] = dot2(nw[j], sp << 16, 0.f); }
#pragma unroll
    for (int j = 0; j < 4; j++) { auto r = __builtin_amdgcn_permlane32_swap(__float_as_uint(v[j]), __float_as_uint(v[j + 4]), false, false); v[j] = __uint_as_float(r[0]) + __uint_as_float(r[1]); }
#pragma unroll
    for (int j = 0; j < 2; j++) { auto r = __builtin_amdgcn_permlane16_swap(__float_as_uint(v[j]), __float_as_uint(v[j + 2]), false, false); v[j] = __uint_as_float(r[0]) + __uint_as_float(r[1]); }
    const float keep = (l & 8) ? v[1] : v[0], give = (l & 8) ? v[0] : v[1];
    LRF(lds0 + w * 256 + ((l & 7) * 8 + (l >> 3)) * 4, L_RED2) = keep + __builtin_bit_cast(float, __builtin_amdgcn_update_dpp(0, __builtin_bit_cast(int, give), 0x128, 0xf, 0xf, true));
  }
  SB();
  __builtin_amdgcn_s_setprio(0);
  XLOAD(5);
  STAMP(1);
  const unsigned dtt = xcd * 8 + (rr >> 2), dcs = rr & 3;
  const bool erole = b >= 32 && b < 144, frole = b >= 144 && b < 176;
  const unsigned eb = b - 32, fb = b - 144;
  const unsigned rho = l & 15, kgp = l >> 4;
  const unsigned amask = kgp == (rho >> 2) ? 0xffffu << (16 * (rho & 1)) : 0u;
  unsigned m0 = (rho & 2) ? 0u : amask, m1 = (rho & 2) ? amask : 0u;
  unsigned mwb = lds0 + L_MODW + w * 256;
  unsigned ca1 = mwb + (8 * kgp + (rho & 3)) * 2, mwl = mwb + l * 2, mws = mwb + kgp * 16;
  unsigned one = rho == 0 ? 0x3F803F80u : 0u, srow = rho == 1 ? 0xffffffffu : 0u;
  unsigned wpb = v16 + L_WP + w * 6144;
  PIN(m0); PIN(m1); PIN(ca1); PIN(mwl); PIN(mws); PIN(one); PIN(srow); PIN(wpb);
  STAMP(3);
  {
    unsigned bad = 0;
#pragma unroll
    for (int i = 0; i < 4; i++) bad |= gran_bad(mg[i][0], mg[i][1], t32, t8);
    if (__builtin_expect(__any(bad != 0), 0)) {
      unsigned spins = 0;
      _Pragma("clang loop unroll(disable)") do {
        bad = 0;
#pragma unroll
        for (int i = 0; i < 4; i++) { mg[i] = ld8_sc1(ws, l * 8, smg + (i & 1) * 8192 + (i >> 1) * 4096); }
#pragma unroll
        for (int i = 0; i < 4; i++) bad |= gran_bad(mg[i][0], mg[i][1], t32, t8);
      } while (__any(bad != 0) && !spin_fail(spins, ws));
    }
  }
  {
    const float sa = gval(mg[0][0]) + gval(mg[1][0]);
    const float ca = rbf(gval(mg[2][0]) + gval(mg[3][0]));
    const unsigned pk = cvtpk(1.f + ca, sa);
    *(LAS u16*)(lp(mwl)) = (u16)pk;
    *(LAS u16*)(lp(mwl) + 128) = (u16)(pk >> 16);
  }
  SB();
  __builtin_amdgcn_s_setprio(2);
  unsigned wrb = lds0 + L_WRAW + w * 6144 + l16;
  PIN(wrb);
#pragma unroll
  for (int m = 0; m < 3; m++)
#pragma unroll
    for (int i = 0; i < 2; i++) LR16(wrb, m * 2048 + i * 1024) = wl[m][i];
  v4u wraw[3][2];
  {
    unsigned trb = lds0 + L_WRAW + w * 6144 + (8 * (l >> 4) + ((l & 15) >> 2)) * 32 + (l & 3) * 8;
    PIN(trb);
#pragma unroll
    for (int m = 0; m < 3; m++)
#pragma unroll
      for (int s = 0; s < 2; s++) {
        const v2u t0 = lds_tr(trb, m * 2048 + s * 1024), t1 = lds_tr(trb, m * 2048 + s * 1024 + 128);
        wraw[m][s] = v4u{t0[0], t0[1], t1[0], t1[1]};
      }
  }
  stw0 += L_WRAW - L_WP; stw1 += L_WRAW - L_WP; str0 += L_WRAW - L_WP; str1 += L_WRAW - L_WP;
  PIN(stw0); PIN(stw1); PIN(str0); PIN(str1);
  STAMP(2);
  STAMP(4);
  {
    const v4u Aone = {one, one, one, one};
    v4u A1[2], A2[2], As[2];
#pragma unroll
    for (int s = 0; s < 2; s++) {
      unsigned v1 = *(LAS u16*)(lp(ca1) + 64 * s), v2 = *(LAS u16*)(lp(ca1) + 64 * s + 8);
      As[s] = LR16(mws, 128 + 64 * s);
      v1 |= v1 << 16; v2 |= v2 << 16;
      A1[s] = v4u{v1 & m0, v1 & m1, 0u, 0u}; A2[s] = v4u{0u, 0u, v2 & m0, v2 & m1};
      As[s] = v4u{As[s][0] & srow, As[s][1] & srow, As[s][2] & srow, As[s][3] & srow};
    }
    __builtin_amdgcn_s_setprio(1);
    const f32x4 z = {0, 0, 0, 0};
    f32x4 D1[2][3], D2[2][3], st[3];
#pragma unroll
    for (int s = 0; s < 2; s++)
#pragma unroll
      for (int m = 0; m < 3; m++) { D1[s][m] = mfma16(A1[s], wraw[m][s], z); D2[s][m] = mfma16(A2[s], wraw[m][s], z); }
#pragma unroll
    for (int m = 0; m < 3; m++) st[m] = mfma16(As[1], wraw[m][1], mfma16(As[0], wraw[m][0], z));
    SB();
#pragma unroll
    for (int s = 0; s < 2; s++)
#pragma unroll
      for (int m = 0; m < 3; m++) {
        const v4u wp = {cvtpk(D1[s][m][0], D1[s][m][1]), cvtpk(D1[s][m][2], D1[s][m][3]), cvtpk(D2[s][m][0], D2[s][m][1]), cvtpk(D2[s][m][2], D2[s][m][3])};
        LR16(wpb, (s * 3 + m) * 1024) = wp;
        st[m] = mfma16(Aone, wp, st[m]);
      }
    __builtin_amdgcn_s_setprio(0);
    if (l < 16) {
      const unsigned ab = lds0 + L_AUG + l * 16 + w * 2;
#pragma unroll
      for (int m = 0; m < 3; m++) {
        const unsigned hi = cvtpk(st[m][0], st[m][1]);
        const unsigned lo = cvtpk(st[m][1] - hi2f(hi), st[m][0] - lo2f(hi));
        *(LAS u16*)(lp(ab) + m * 1024) = (u16)hi; *(LAS u16*)(lp(ab) + m * 1024 + 256) = (u16)(hi >> 16);
        *(LAS u16*)(lp(ab) + m * 1024 + 512) = (u16)lo; *(LAS u16*)(lp(ab) + m * 1024 + 768) = (u16)(lo >> 16);
      }
    }
  }
  STAMP(5);
  SB();
  XLOAD(6);
  __syncthreads();
  if (w == 7) {
    float s_ = LRF(lds0 + l * 4, L_RED2);
#pragma unroll
    for (int i = 1; i < 8; i++) s_ += LRF(lds0 + l * 4, L_RED2 + i * 256);
    st8_sc1(ws, l * 8, WS_MODP + ((b & 7) * 2048 + (b >> 3) * 64) * 8, granule(t32, t8, s_));
  }
  STAMP(6);
  f32x4 aq[2], ak[2], av[2];
  unsigned wpr = v16 + L_WP;
  PIN(wpr);
#define GMMA(wq, wk, wv, x0_, x1_, first)                                               \
  {                                                                                     \
    const f32x4 z = {0, 0, 0, 0};                                                       \
    aq[0] = mfma16(wq, x0_, (first) ? z : aq[0]); ak[0] = mfma16(wk, x0_, (first) ? z : ak[0]); av[0] = mfma16(x0_, wv, (first) ? z : av[0]); \
    aq[1] = mfma16(wq, x1_, (first) ? z : aq[1]); ak[1] = mfma16(wk, x1_, (first) ? z : ak[1]); av[1] = mfma16(x1_, wv, (first) ? z : av[1]); \
  }
  v4u wc[2][3], wn[2][3];
#define WLOAD(dst, p) { _Pragma("unroll") for (int s_ = 0; s_ < 2; s_++) _Pragma("unroll") for (int m_ = 0; m_ < 3; m_++) dst[s_][m_] = LR16(wpr, ((2 * (p) + s_) * 3 + m_) * 1024); }
#define GPAIR(src, p) { GMMA(src[0][0], src[0][1], src[0][2], xf[0][2 * (p)], xf[1][2 * (p)], (p) == 0); GMMA(src[1][0], src[1][1], src[1][2], xf[0][2 * (p) + 1], xf[1][2 * (p) + 1], false); }
  XLOAD(7);
  WLOAD(wc, 0);
  const unsigned sln = WS_LNST + (256 * qb + 32 * w) * 16;
  v4u lg0 = ld16_sc1(ws, (l & 15) * 16, sln), lg1 = ld16_sc1(ws, (l & 15) * 16, sln + 256);
  SB();
  const v4u sentv = {SENT, SENT, SENT, SENT};
  st16_sc1(ws, l16, WS_KF + (h * 64 + qb * 8 + w) * 1024, sentv);
  st16_sc1(ws, l16, WS_VF + (h * 64 + qb * 8 + w) * 1024, sentv);
  SB();
  __builtin_amdgcn_s_setprio(3); WLOAD(wn, 1); SB(); GPAIR(wc, 0); SB();
  WLOAD(wc, 2); SB(); GPAIR(wn, 1); SB();
  __builtin_amdgcn_s_setprio(2); WLOAD(wn, 3); SB(); GPAIR(wc, 2); SB();
  XPIECE(4); WLOAD(wc, 4); SB(); GPAIR(wn, 3); SB();
  __builtin_amdgcn_s_setprio(1); XPIECE(5); WLOAD(wn, 5); SB(); GPAIR(wc, 4); SB();
  XPIECE(6); WLOAD(wc, 6); SB(); GPAIR(wn, 5); SB();
  __builtin_amdgcn_s_setprio(0); XPIECE(7); WLOAD(wn, 7); SB();
  GPAIR(wc, 6); SB();
  GPAIR(wn, 7);
  {
    unsigned bad = gran_bad(lg0[0], lg0[1], t32, t8) | gran_bad(lg0[2], lg0[3], t32, t8) | gran_bad(lg1[0], lg1[1], t32, t8) | gran_bad(lg1[2], lg1[3], t32, t8);
    if (__builtin_expect(__any(bad != 0), 0)) {
      unsigned spins = 0;
      _Pragma("clang loop unroll(disable)") do {
        lg0 = ld16_sc1(ws, (l & 15) * 16, sln); lg1 = ld16_sc1(ws, (l & 15) * 16, sln + 256);
        bad = gran_bad(lg0[0], lg0[1], t32, t8) | gran_bad(lg0[2], lg0[3], t32, t8) | gran_bad(lg1[0], lg1[1], t32, t8) | gran_bad(lg1[2], lg1[3], t32, t8);
      } while (__any(bad != 0) && !spin_fail(spins, ws));
    }
  }
  asm volatile("s_waitcnt vmcnt(0)" ::: "memory");
  st8_sc1(ws, l == 0 ? 0u : 0x80000000u, WS_KVF + (h * 8 + qb) * 128 + w * 8, v2u{(unsigned)ep, (unsigned)(ep >> 32)});
  STAMP(7);
  v2u mp = ld8_sc1(ws, (l >> 3) * 16384 + (l & 7) * 8, WS_MODP + b * 64);
  SB();
  v4u qfrag;
  {
    const unsigned g = l >> 4, t = l & 15;
    const bool cs_row = g == 0 || g == 3;
    unsigned xa[2]; float rs[2];
#pragma unroll
    for (int u = 0; u < 2; u++) {
      const unsigned mb = (u ? lg1[0] : lg0[0]) & 0xffff0000u, sb_ = (u ? lg1[2] : lg0[2]) & 0xffff0000u;
      const unsigned v = cs_row ? mb ^ 0x80000000u : sb_;
      xa[u] = v | (v >> 16);
      rs[u] = frcp(__uint_as_float(sb_));
    }
    unsigned lnb = lds0 + L_LN + w * 128;
    LRF(lnb + t * 4, 0) = rs[0]; LRF(lnb + t * 4, 64) = rs[1];
    {
      const v4u wq = LR16(v16, L_AUG), wk = LR16(v16, L_AUG + 1024), wv = LR16(v16, L_AUG + 2048);
      const v4u x0_ = {xa[0], xa[0], xa[0], xa[0]}, x1_ = {xa[1], xa[1], xa[1], xa[1]};
      GMMA(wq, wk, wv, x0_, x1_, false);
    }
    const f32x4 rv0 = LRF4(lnb + g * 16, 0), rv1 = LRF4(lnb + g * 16, 64);
    const unsigned fo = (g >> 1) * 512 + t * 16 + (g & 1) * 8;
    unsigned kt = lds0 + L_K + w * 1024 + fo, qt = lds0 + L_QS + w * 1024 + fo;
    unsigned vt = lds0 + L_V + 2 * w * VG + (g >> 1) * 256 + t * 16 + (g & 1) * 8;
    PIN(kt); PIN(vt);
#pragma unroll
    for (int u = 0; u < 2; u++) {
      const float rq = rs[u] * QSCALE;
      const f32x4 rv = u ? rv1 : rv0;
      LR8(qt, u * 256) = v2u{cvtpk(aq[u][0] * rq, aq[u][1] * rq), cvtpk(aq[u][2] * rq, aq[u][3] * rq)};
      LR8(kt, u * 256) = v2u{cvtpk(ak[u][0] * rs[u], ak[u][1] * rs[u]), cvtpk(ak[u][2] * rs[u], ak[u][3] * rs[u])};
      LR8(vt, u * VG) = v2u{cvtpk(av[u][0] * rv[0], av[u][1] * rv[1]), cvtpk(av[u][2] * rv[2], av[u][3] * rv[3])};
    }
    qfrag = LR16(v16, L_QS + w * 1024);
    unsigned kp = v16 + L_K + w * 1024, vp = v16 + L_V + w * 1024;
    const v4u kpub = LR16(kp, 0), vpub = LR16(vp, 0);
    st16_sc1(ws, l16, WS_KF + (h * 64 + qb * 8 + w) * 1024, v4u{nosent(kpub[0]), nosent(kpub[1]), nosent(kpub[2]), nosent(kpub[3])});
    st16_sc1(ws, l16, WS_VF + (h * 64 + qb * 8 + w) * 1024, v4u{nosent(vpub[0]), nosent(vpub[1]), nosent(vpub[2]), nosent(vpub[3])});
  }
  if (tid < 8) *(LAS unsigned*)(lp(lds0 + tid * 4) + L_CNT) = 0u;
  STAMP(8);
  __syncthreads();
  STAMP(9);
  PIN(mp);
  f32x16 O, SA, SB_;
  const unsigned i32 = l & 31, ha = l >> 5;
  const unsigned vrel = ha * 256 + (l & 15) * 16;
  const unsigned vcon = (i32 == 16 || i32 == 28) ? 0x3F803F80u : 0u;
  const unsigned long long vmask = 0x0000FFFF0000FFFFull;
  unsigned ka = v16 + L_K, va = lds0 + L_V + vrel, va2 = va + 64 * VG;
  const unsigned idxv = 0xDDDD;
  const unsigned skb = WS_KF + (h * 64 + w) * 1024;
  unsigned kw = v16 + L_K + w * 1024, vw = v16 + L_V + w * 1024;
  v4u kf, a0, a1, b0, b1, s0k, s0v, s1k, s1v; unsigned tmpv;
  unsigned sc_; u64 sp_;
  const rsrc_t wv2 = mkrs(a.ws + (2u << 20), WS_BYTES - (2u << 20));
  unsigned kq[8];
#pragma unroll
  for (int j = 1; j < 8; j++) kq[j] = skb + ((qb + j) & 7) * 8192;
  {
    const unsigned vfl = WS_KVF + h * 1024 + (l >> 3) * 128 + (l & 7) * 8, elo = (unsigned)ep, ehi = (unsigned)(ep >> 32);
    v2u fl;
    asm volatile(ATTN_A
      : "=&{v[0:15]}"(O), "=&{v[16:31]}"(SA), "=&{v[32:47]}"(SB_), "=&{v[48:51]}"(kf), "=&{v[52:55]}"(a0), "=&{v[56:59]}"(a1), "=&{v[60:63]}"(b0), "=&{v[64:67]}"(b1),
        "=&{v92}"(tmpv), "=&{v[76:77]}"(fl), "=&{v[100:103]}"(s0k), "=&{v[104:107]}"(s0v), "=&{v[108:111]}"(s1k), "=&{v[112:115]}"(s1v),
        [sc] "=&s"(sc_), [sp] "=&s"(sp_)
      : "{v[68:71]}"(qfrag), "{v72}"(idxv), "{v73}"(ka), "{v74}"(va), "{v75}"(va2), "{v94}"(l16), "{v95}"(kw), "{v91}"(vcon), "{v96}"(vw), "{v99}"(vfl), [vm] "s"(vmask),
        [ws] "s"(ws), [wv2] "s"(wv2), [k1] "s"(kq[1]), [k2] "s"(kq[2]), [elo] "s"(elo), [ehi] "s"(ehi)
      : "memory", "scc", "vcc");
  }
  STAMP(10);
  if (w == 0) {
    unsigned spins = 0;
    _Pragma("clang loop unroll(disable)") while (__builtin_expect(__any(gran_bad(mp[0], mp[1], t32, t8) != 0), 0) && !spin_fail(spins, ws)) mp = ld8_sc1(ws, (l >> 3) * 16384 + (l & 7) * 8, WS_MODP + b * 64);
    const float t = swap32_add(swap16_add(dpp_add<0x128>(gval(mp[0]))));
    st8_sc1(ws, l < 8 ? l * 8 : 0x80000000u, WS_MODN + b * 64, granule(t32, t8, rbf(t)));
  } else {
    st8_sc1(ws, 0x80000000u, 0, v2u{0u, 0u});
  }
  asm volatile("s_waitcnt lgkmcnt(0)" ::: "memory");
  __syncthreads();
  STAMP(13);
  STAMPC(14);
  const unsigned ct = w & 3, kh = w >> 2;
  const unsigned c0 = 128 * dcs + 32 * ct + 16 * kh + 4 * ha;
  v16u wonA, wonB, wonC, wonD;
  {
    const rsrc_t rwo = mkrs(a.wo, 512u * 1024u), rwr = mkrs(a.wr, 1024);
    const rsrc_t refa = erole ? mkrs(a.wg, 512u * 3584u) : mkrs(a.wd, frole ? 1792u * 1024u : 0u);
    const rsrc_t refb = erole ? mkrs(a.wu, 512u * 3584u) : refa;
    const rsrc_t refc = mkrs(a.wd, frole ? 1792u * 1024u : 0u);
    unsigned swo = kh * (256 * 1024) + dcs * 256 + ct * 64;
    const unsigned vwo = (l >> 2) * 1024 + (l & 3) * 16;
    const unsigned estr = erole ? 3584u : 1024u;
    const unsigned vef = (l >> 1) * estr + (l & 1) * 16;
    unsigned sef = erole ? w * (64 * 3584) + eb * 32 : w * (224 * 1024) + fb * 32;
    const unsigned sefs = 32 * estr, sefr = erole ? 64 * 3584 : 0u;
    const unsigned vxr = i32 * 1024 + c0 * 2, vwr = c0 * 2, sxr = dtt * 32768;
    v16u efA; v4u ef4, ef5, ef6, wrall, pf0, pf1; v2u xr0, xr1, wr0, wr1;
    const v2u epv = {(unsigned)ep, (unsigned)(ep >> 32)};
    const unsigned spf = WS_PREF, sfo = WS_PREF + b * 8;
    const unsigned sof = WS_OF + ((qb * 8 + w) * 32 + h) * 1024;
    unsigned vmi = 0x80000000u, smi = 0;
    if (w == 4) { vmi = l < 32 ? l16 : vmi; smi = WS_ST + (dtt * 4 + dcs) * 512; }
    if (w == 5) { vmi = (l >> 4) * 1024 + (l & 15) * 16; smi = WS_ROWS + dtt * 4096 + dcs * 256; }
    if (w == 6 && dcs == 0) { vmi = l < 8 ? l * 1024 : vmi; smi = WS_CAND + dtt * 16; }
    if (w == 7 && erole) { vmi = l < 16 ? (l >> 2) * 7168 + ((l >> 1) & 1) * 3584 + (l & 1) * 16 : vmi; smi = WS_GU + eb * 32; }
    v4u sent_tmp; unsigned cntt;
    const unsigned cnta = lds0 + L_CNT, one = 1;
    asm volatile(ATTN_B
      : "+{v[0:15]}"(O), "=&{v[16:31]}"(SA), "=&{v[32:47]}"(SB_), "=&{v[48:51]}"(kf), "=&{v[52:55]}"(a0), "=&{v[56:59]}"(a1), "=&{v[60:63]}"(b0), "=&{v[64:67]}"(b1),
        "+{v[100:103]}"(s0k), "+{v[104:107]}"(s0v), "=&{v[108:111]}"(s1k), "=&{v[112:115]}"(s1v),
        "=&{v[128:143]}"(wonA), "=&{v[144:159]}"(wonB), "=&{v[160:175]}"(wonC), "=&{v[176:191]}"(wonD),
        "=&{v[192:207]}"(efA), "=&{v[208:211]}"(ef4), "=&{v[212:215]}"(ef5), "=&{v[216:219]}"(ef6),
        "=&{v[220:221]}"(xr0), "=&{v[222:223]}"(xr1), "=&{v[224:225]}"(wr0), "=&{v[226:227]}"(wr1), "=&{v[228:231]}"(wrall), "=&{v[232:235]}"(pf0), "=&{v[236:239]}"(pf1), "=&{v92}"(tmpv), "=&{v[84:87]}"(sent_tmp), "=&{v93}"(cntt),
        [swo] "+s"(swo), [sef] "+s"(sef), [sc] "=&s"(sc_), [sp] "=&s"(sp_)
      : "{v[68:71]}"(qfrag), "{v72}"(idxv), "{v73}"(ka), "{v74}"(va), "{v75}"(va2), "{v[78:79]}"(epv), "{v88}"(vmi), "{v89}"(cnta), "{v90}"(one), "{v91}"(vcon), [vm] "s"(vmask), "{v94}"(l16), "{v95}"(kw), "{v96}"(vw), "{v97}"(vwo), "{v98}"(vef),
        "{v123}"(vxr), "{v124}"(vwr),
        [ws] "s"(ws), [rwo] "s"(rwo), [refa] "s"(refa), [refb] "s"(refb), [refc] "s"(refc), [wv2] "s"(wv2), [k2] "s"(kq[2]), [k3] "s"(kq[3]), [k4] "s"(kq[4]), [k5] "s"(kq[5]), [k6] "s"(kq[6]), [k7] "s"(kq[7]), [sefs] "s"(sefs), [sefr] "s"(sefr),
        [rx] "s"(rx), [rwr] "s"(rwr), [sxr] "s"(sxr), [spf] "s"(spf), [sfo] "s"(sfo), [wv] "s"(w), [sof] "s"(sof), [smi] "s"(smi)
      : "memory", "scc", "vcc");
    STAMPC(15);
    STAMP(16);
    {
      float lsum = ha ? O[12] : O[8];
      const float mx = fmaxf(fmaxf(fmaxf(fabsf(O[0]), fabsf(O[1])), fmaxf(fabsf(O[2]), fabsf(O[3]))), fmaxf(fmaxf(fabsf(O[4]), fabsf(O[5])), fmaxf(fabsf(O[6]), fabsf(O[7]))));
      const bool okl = lsum >= 5.421010862427522e-20f && lsum <= 1.8446744073709552e19f && mx <= 3.0e38f;
      if (__builtin_expect(!__all(okl), 0)) {
        float m = -3.0e38f;
        const f32x16 z = {0.f};
        f32x16 negm;
        for (unsigned kt = 0; kt < 64; kt++) {
          const f32x16 s = mfma32(LR16(ka + kt * 1024, 0), qfrag, z);
#pragma unroll
          for (int i = 0; i < 16; i++) m = fmaxf(m, s[i]);
        }
        m = swap32_max(m);
#pragma unroll
        for (int i = 0; i < 16; i++) negm[i] = -m;
        unsigned ka_ = ka, va_ = va; int n = 8;
        asm volatile(ATTN_F
          : "=&{v[0:15]}"(O), "=&{v[16:31]}"(SA), "=&{v[32:47]}"(SB_), "=&{v[48:51]}"(kf), "=&{v[52:55]}"(a0), "=&{v[56:59]}"(a1), "=&{v[60:63]}"(b0), "=&{v[64:67]}"(b1),
            "+{v73}"(ka_), "+{v74}"(va_), [n] "+s"(n)
          : "{v[68:71]}"(qfrag), "{v72}"(idxv), "{v91}"(vcon), "{v[100:115]}"(negm), [vm] "s"(vmask) : "memory", "scc");
        lsum = ha ? O[12] : O[8];
      }
      const float inv = frcp(lsum);
      const v4u o = {nosent(cvtpk(O[0] * inv, O[1] * inv)), nosent(cvtpk(O[2] * inv, O[3] * inv)), nosent(cvtpk(O[4] * inv, O[5] * inv)), nosent(cvtpk(O[6] * inv, O[7] * inv))};
      st16_sc1(ws, l16, WS_OF + ((qb * 8 + w) * 32 + h) * 1024, o);
    }
  {
      const unsigned elo = (unsigned)ep, ehi = (unsigned)(ep >> 32);
#define PFBAD ((pf0[0] ^ elo) | (pf0[1] ^ ehi) | (pf0[2] ^ elo) | (pf0[3] ^ ehi) | (pf1[0] ^ elo) | (pf1[1] ^ ehi) | (pf1[2] ^ elo) | (pf1[3] ^ ehi))
      if (__builtin_expect(__any(PFBAD != 0), 0)) {
        unsigned spins = 0;
        _Pragma("clang loop unroll(disable)") do { pf0 = ld16_sc1(ws, l16, WS_PREF); pf1 = ld16_sc1(ws, l16, WS_PREF + 1024); } while (__any(PFBAD != 0) && !spin_fail(spins, ws));
      }
    }
    STAMP(17);
    v4u g0 = ld16_sc1(ws, tid * 32, WS_MODN), g1 = ld16_sc1(ws, tid * 32 + 16, WS_MODN);
    SB();
    unsigned priv = v16 + L_QS + w * 1024;
    unsigned tra = lds0 + L_QS + w * 1024 + (4 * ha + ((l & 15) >> 2)) * 64 + (((l >> 4) & 1) ^ kh) * 32 + (l & 3) * 8;
    unsigned trb = lds0 + L_QS + w * 1024 + (8 * (l >> 4) + ((l & 15) >> 2)) * 32 + (l & 3) * 8;
    v4u woA[16], efB[7];
#pragma unroll
    for (int i = 0; i < 16; i++) {
      const v16u& src = i < 4 ? wonA : i < 8 ? wonB : i < 12 ? wonC : wonD;
      LR16(priv, 0) = v4u{src[4 * (i & 3)], src[4 * (i & 3) + 1], src[4 * (i & 3) + 2], src[4 * (i & 3) + 3]};
      const v2u t0 = lds_tr(tra, 0), t1 = lds_tr(tra, 512);
      woA[i] = v4u{t0[0], t0[1], t1[0], t1[1]};
    }
    if (erole || frole) {
#pragma unroll
      for (int i = 0; i < 7; i++) {
        LR16(priv, 0) = i < 4 ? v4u{efA[4 * i], efA[4 * i + 1], efA[4 * i + 2], efA[4 * i + 3]} : i == 4 ? ef4 : i == 5 ? ef5 : ef6;
        const v2u t0 = lds_tr(trb, 0), t1 = lds_tr(trb, 128);
        efB[i] = v4u{t0[0], t0[1], t1[0], t1[1]};
      }
    }
    v4u og[4];
    const unsigned sog = WS_OF + (dtt * 32 + 4 * w) * 1024;
#pragma unroll
    for (int i = 0; i < 4; i++) og[i] = ld16_sc1(ws, l16, sog + i * 1024);
    SB();
    {
      unsigned bad = gran_bad(g0[0], g0[1], t32, t8) | gran_bad(g0[2], g0[3], t32, t8) | gran_bad(g1[0], g1[1], t32, t8) | gran_bad(g1[2], g1[3], t32, t8);
      if (__builtin_expect(__any(bad != 0), 0)) {
        unsigned spins = 0;
        _Pragma("clang loop unroll(disable)") do {
          g0 = ld16_sc1(ws, tid * 32, WS_MODN); g1 = ld16_sc1(ws, tid * 32 + 16, WS_MODN);
          bad = gran_bad(g0[0], g0[1], t32, t8) | gran_bad(g0[2], g0[3], t32, t8) | gran_bad(g1[0], g1[1], t32, t8) | gran_bad(g1[2], g1[3], t32, t8);
        } while (__any(bad != 0) && !spin_fail(spins, ws));
      }
      LR16(lds0 + tid * 16, L_MODN) = v4u{g0[0] & 0xffff0000u, g0[2] & 0xffff0000u, g1[0] & 0xffff0000u, g1[2] & 0xffff0000u};
    }
    SB();
    STAMP(18);
    __syncthreads();
    float xv[8], gav[8], c2v[8], sbv[8], gbv[8], vr[8];
    {
      unsigned mb_ = lds0 + L_MODN + c0 * 4;
#pragma unroll
      for (int j = 0; j < 2; j++) {
        const f32x4 ga4 = LRF4(mb_, 32 * j), sb4 = LRF4(mb_, 2048 + 32 * j), cb4 = LRF4(mb_, 4096 + 32 * j), gb4 = LRF4(mb_, 6144 + 32 * j);
        const v2u xr = j ? xr1 : xr0, wr = j ? wr1 : wr0;
#pragma unroll
        for (int r = 0; r < 4; r += 2) {
          const unsigned c2p = cvtpk(1.f + cb4[r], 1.f + cb4[r + 1]);
          c2v[4 * j + r] = lo2f(c2p); c2v[4 * j + r + 1] = hi2f(c2p);
          xv[4 * j + r] = lo2f(xr[r >> 1]); xv[4 * j + r + 1] = hi2f(xr[r >> 1]);
          vr[4 * j + r] = c2v[4 * j + r] * lo2f(wr[r >> 1]); vr[4 * j + r + 1] = c2v[4 * j + r + 1] * hi2f(wr[r >> 1]);
        }
#pragma unroll
        for (int r = 0; r < 4; r++) { gav[4 * j + r] = ga4[r]; sbv[4 * j + r] = sb4[r]; gbv[4 * j + r] = gb4[r]; }
      }
    }
#pragma unroll
    for (int i = 0; i < 8; i++) { PIN(xv[i]); PIN(gav[i]); PIN(c2v[i]); PIN(sbv[i]); PIN(gbv[i]); PIN(vr[i]); }
    float cvs = 0.f, cbs = 0.f;
    if (w == 0) {
      const unsigned mb_ = lds0 + L_MODN + l * 32;
      const f32x4 ca_ = LRF4(mb_, 4096), cb_ = LRF4(mb_, 4096 + 16), sa_ = LRF4(mb_, 2048), sb_ = LRF4(mb_, 2048 + 16);
#pragma unroll
      for (int j = 0; j < 8; j += 2) {
        const f32x4& cc = j < 4 ? ca_ : cb_; const f32x4& ss = j < 4 ? sa_ : sb_;
        const unsigned c2p = cvtpk(1.f + cc[j & 3], 1.f + cc[(j & 3) + 1]);
        const float w0 = lo2f(wrall[j >> 1]), w1 = hi2f(wrall[j >> 1]);
        cvs = fmaf(lo2f(c2p), w0, fmaf(hi2f(c2p), w1, cvs));
        cbs = fmaf(ss[j & 3], w0, fmaf(ss[(j & 3) + 1], w1, cbs));
      }
      cvs = wave_sum(cvs); cbs = wave_sum(cbs);
      PIN(cvs); PIN(cbs);
    }
    {
      unsigned miss = 0;
#pragma unroll
      for (int i = 0; i < 4; i++) miss |= __any(umax4(og[i]) == SENT) ? 1u << i : 0u;
      unsigned spins = 0;
      _Pragma("clang loop unroll(disable)") while (__builtin_expect(miss != 0, 0) && !spin_fail(spins, ws)) {
#pragma unroll
        for (int i = 0; i < 4; i++) if (miss & (1u << i)) og[i] = ld16_sc1(ws, l16, sog + i * 1024);
        unsigned m2 = 0;
#pragma unroll
        for (int i = 0; i < 4; i++) m2 |= __any(umax4(og[i]) == SENT) ? 1u << i : 0u;
        miss = m2;
      }
    }
    STAMP(19);
    unsigned ost = v16 + L_OST;
    PIN(ost);
#pragma unroll
    for (int i = 0; i < 4; i++) LR16(ost + w * 4096, i * 1024) = og[i];
    __syncthreads();
    f32x16 da;
    {
      const unsigned osr = ost + kh * 16384;
      v4u ob[16];
#pragma unroll
      for (int i = 0; i < 16; i++) ob[i] = LR16(osr, i * 1024);
      SB();
      const f32x16 z = {0.f};
#pragma unroll
      for (int i = 0; i < 16; i++) da = mfma32(woA[i], ob[i], i ? da : z);
    }
    unsigned xch = v16 + L_XCH;
    PIN(xch);
    LRF4(xch + w * 2048, 0) = f32x4{da[8], da[9], da[10], da[11]}; LRF4(xch + w * 2048, 1024) = f32x4{da[12], da[13], da[14], da[15]};
    float own[8];
#pragma unroll
    for (int i = 0; i < 8; i++) own[i] = da[i];
    __syncthreads();
    float x1[8];
    {
      const f32x4 p0 = LRF4(xch + (w ^ 4) * 2048, 0), p1 = LRF4(xch + (w ^ 4) * 2048, 1024);
      float s1 = 0.f, s2 = 0.f, s3 = 0.f;
#pragma unroll
      for (int i = 0; i < 8; i++) {
        const float xx = fmaf(gav[i], own[i] + (i < 4 ? p0[i & 3] : p1[i & 3]), xv[i]);
        x1[i] = xx; s1 += xx; s2 = fmaf(xx, xx, s2); s3 = fmaf(xx, vr[i], s3);
      }
      s1 = swap32_add(s1); s2 = swap32_add(s2); s3 = swap32_add(s3);
      const unsigned pa = lds0 + i32 * 32 + w * 4;
      LRF(pa, L_PST) = s1; LRF(pa, L_PST + 1024) = s2; LRF(pa, L_PST + 2048) = s3;
    }
    __syncthreads();
    STAMP(20);
    if (w == 0) {
      const unsigned pr = lds0 + i32 * 32 + ha * 16;
      const f32x4 u1 = LRF4(pr, L_PST), u2 = LRF4(pr, L_PST + 1024), u3 = LRF4(pr, L_PST + 2048);
      const float q1 = swap32_add((u1[0] + u1[1]) + (u1[2] + u1[3])), q2 = swap32_add((u2[0] + u2[1]) + (u2[2] + u2[3])), q3 = swap32_add((u3[0] + u3[1]) + (u3[2] + u3[3]));
      st16_sc1(ws, l < 32 ? l16 : 0x80000000u, WS_ST + (dtt * 4 + dcs) * 512, v4u{nosent(__float_as_uint(q1)), nosent(__float_as_uint(q2)), nosent(__float_as_uint(q3)), 0u});
      v4u pa, pb;
      unsigned spins = 0;
      _Pragma("clang loop unroll(disable)") do {
        pa = ld16_sc1(ws, l16, WS_ST + dtt * 2048); pb = ld16_sc1(ws, l16, WS_ST + dtt * 2048 + 1024);
      } while (__any(umax4(pa) == SENT || umax4(pb) == SENT) && !spin_fail(spins, ws));
      const float t1 = swap32_add(__uint_as_float(pa[0]) + __uint_as_float(pb[0]));
      const float t2 = swap32_add(__uint_as_float(pa[1]) + __uint_as_float(pb[1]));
      const float t3 = swap32_add(__uint_as_float(pa[2]) + __uint_as_float(pb[2]));
      const float mean = t1 * (1.f / 512.f);
      const float var = fmaxf(t2 * (1.f / 512.f) - mean * mean, 0.f);
      const float mb = rbf(mean), rs = frcp(rbf(__builtin_amdgcn_sqrtf(rbf(rbf(var) + 1e-5f))));
      const float rrv = fmaf(t3 - mb * cvs, rs, cbs);
      const unsigned key = mkkey(rrv, 32 * dtt + i32);
      const unsigned k1 = half_umax(key), k2 = half_umax(key == k1 ? 0u : key);
      if (dcs == 0) st16_sc1(ws, l < 8 ? l * 1024 : 0x80000000u, WS_CAND + dtt * 16, v4u{k1, k2, 0u, 0u});
      *(LAS f32x2*)(lp(lds0 + i32 * 16) + L_TOK) = f32x2{rs, -mb * rs};
      *(LAS v2u*)(lp(lds0) + L_SEL) = v2u{(2047u - (k1 & 2047u)) & 31u, (2047u - (k2 & 2047u)) & 31u};
    }
    __syncthreads();
    STAMP(21);
    const v2u loc = LR8(lds0, L_SEL);
    v2u o0, o1;
    const bool held = i32 == loc[0] || i32 == loc[1];
    const rsrc_t ro = mkrs(a.out, 2u << 20);
    const unsigned voo = i32 * 1024 + c0 * 2, soo = dtt * 32768;
    {
      const f32x2 tk = *(LAS f32x2*)(lp(lds0 + i32 * 16) + L_TOK);
      float h2[8], ot[8];
#pragma unroll
      for (int j = 0; j < 8; j++) { h2[j] = fmaf(fmaf(x1[j], tk[0], tk[1]), c2v[j], sbv[j]); ot[j] = fmaf(gbv[j], h2[j], x1[j]); }
      o0 = v2u{cvtpk(ot[0], ot[1]), cvtpk(ot[2], ot[3])}; o1 = v2u{cvtpk(ot[4], ot[5]), cvtpk(ot[6], ot[7])};
      if (held) {
        const unsigned ro_ = WS_ROWS + dtt * 4096 + (i32 == loc[0] ? 0u : 2048u) + c0 * 2;
        st8_sc1(ws, ro_, 0, v2u{nosent(cvtpk(h2[0], h2[1])), nosent(cvtpk(h2[2], h2[3]))});
        st8_sc1(ws, ro_ + 16, 0, v2u{nosent(cvtpk(h2[4], h2[5])), nosent(cvtpk(h2[6], h2[7]))});
        st8_sc1(ws, ro_ + 1024, 0, v2u{nosent(cvtpk(x1[0], x1[1])), nosent(cvtpk(x1[2], x1[3]))});
        st8_sc1(ws, ro_ + 1040, 0, v2u{nosent(cvtpk(x1[4], x1[5])), nosent(cvtpk(x1[6], x1[7]))});
      }
      const unsigned vo_ = held ? 0x80000000u : voo;
      __builtin_amdgcn_raw_buffer_store_b64(o0, ro, vo_, soo, 0);
      __builtin_amdgcn_raw_buffer_store_b64(o1, ro, vo_ + 16, soo, 0);
    }
    STAMP(22);
    if (w == 0) {
      v4u cd;
      unsigned spins = 0;
      _Pragma("clang loop unroll(disable)") do { cd = ld16_sc1(ws, l16, WS_CAND + xcd * 1024); } while (__any(umax4(cd) == SENT) && !spin_fail(spins, ws));
      const unsigned K1 = swap32_umax(half_umax(cd[0]));
      const unsigned K2 = swap32_umax(half_umax(cd[0] == K1 ? cd[1] : cd[0]));
      const bool second = __any(cd[0] == K1 && cd[1] == K2);
      const unsigned T1 = 2047u - (K1 & 2047u), T2 = 2047u - (K2 & 2047u);
      *(LAS v4u*)(lp(lds0) + L_SEL + 16) = v4u{T1, T2, WS_ROWS + (T1 >> 5) * 4096, WS_ROWS + (T2 >> 5) * 4096 + (second ? 2048u : 0u)};
      *(LAS f32x2*)(lp(lds0) + L_SEL + 32) = f32x2{keyval(K1), keyval(K2)};
    }
    __syncthreads();
    STAMP(23);
    const v4u sel = LR16(lds0, L_SEL + 16);
    if (held) {
      const unsigned tk = 32 * dtt + i32;
      const unsigned vo_ = (tk == sel[0] || tk == sel[1]) ? 0x80000000u : voo;
      __builtin_amdgcn_raw_buffer_store_b64(o0, ro, vo_, soo, 0);
      __builtin_amdgcn_raw_buffer_store_b64(o1, ro, vo_ + 16, soo, 0);
    }
    const unsigned rw_ = l & 15, kg = l >> 4;
    if (erole) {
      const unsigned vxa = rw_ < 2 ? (rw_ ? sel[3] : sel[2]) + kg * 16 : 0x80000000u;
      v4u xa0, xa1;
      unsigned spins = 0;
      _Pragma("clang loop unroll(disable)") do { xa0 = ld16_sc1(ws, vxa, w * 128); xa1 = ld16_sc1(ws, vxa, w * 128 + 64); } while (__any(umax4(xa0) == SENT || umax4(xa1) == SENT) && !spin_fail(spins, ws));
      const f32x4 z = {0.f, 0.f, 0.f, 0.f};
      const f32x4 dg = mfma16(xa1, efB[1], mfma16(xa0, efB[0], z)), du = mfma16(xa1, efB[3], mfma16(xa0, efB[2], z));
      if (l < 16) { const unsigned ea = lds0 + w * 256 + l * 4; LRF(ea, L_ERED) = dg[0]; LRF(ea, L_ERED + 64) = dg[1]; LRF(ea, L_ERED + 128) = du[0]; LRF(ea, L_ERED + 192) = du[1]; }
      __syncthreads();
      if (w == 0) {
        float s_ = LRF(lds0 + l * 4, L_ERED);
#pragma unroll
        for (int i = 1; i < 8; i++) s_ += LRF(lds0 + l * 4, L_ERED + i * 256);
        auto r = __builtin_amdgcn_permlane32_swap(__float_as_uint(s_), __float_as_uint(s_), false, false);
        const float g_ = rbf(__uint_as_float(r[0])), u_ = rbf(__uint_as_float(r[1]));
        const float gu = rbf(g_ * sigm(g_)) * u_;
        const float nb = __builtin_bit_cast(float, __builtin_amdgcn_update_dpp(0, __builtin_bit_cast(int, gu), 0xB1, 0xf, 0xf, true));
        st4_sc1(ws, ((l >> 5) * 2 + (l & 1)) * 7168 + ((l >> 4) & 1) * 3584 + (l & 14) * 2, WS_GU + eb * 32, nosent((l & 1) ? cvtpk(nb, gu) : cvtpk(gu, nb)));
      }
    } else if (frole) {
      const unsigned fc = fb * 16 + rw_, ft = kg & 1;
      const unsigned vhx = (ft ? sel[3] : sel[2]) + (fc & ~1u) * 2;
      unsigned hx, xx;
      {
        unsigned spins = 0;
        _Pragma("clang loop unroll(disable)") do { hx = __builtin_amdgcn_raw_buffer_load_b32(ws, vhx, 0, 16); xx = __builtin_amdgcn_raw_buffer_load_b32(ws, vhx, 1024, 16); }
        while (__any(hx == SENT || xx == SENT) && !spin_fail(spins, ws));
      }
      float sg = rbf(sigm(rbf(LRF(lds0 + ft * 4, L_SEL + 32))));
      float hv = (fc & 1) ? hi2f(hx) : lo2f(hx), xv_ = (fc & 1) ? hi2f(xx) : lo2f(xx), gbf = LRF(lds0 + fc * 4, L_MODN + 6144);
      PIN(sg); PIN(hv); PIN(xv_); PIN(gbf);
      const unsigned lt = l >= 28 ? 1u : 0u, lj = l - 28 * lt;
      const unsigned vga = l < 56 ? lt * 3584 + lj * 16 : 0x80000000u;
      v4u gv;
      {
        unsigned spins = 0;
        _Pragma("clang loop unroll(disable)") do { gv = ld16_sc1(ws, vga, WS_GU + (fb & 3) * 7168 + w * 448); } while (__any(umax4(gv) == SENT) && !spin_fail(spins, ws));
      }
      LR16(v16, L_QS + w * 1024) = gv;
      const unsigned gar = lds0 + L_QS + w * 1024 + (rw_ & 1) * 448 + kg * 16;
      f32x4 dy = {0.f, 0.f, 0.f, 0.f};
#pragma unroll
      for (int s = 0; s < 7; s++) dy = mfma16(LR16(gar, s * 64), efB[s], dy);
      if (l < 16) { const unsigned ea = lds0 + w * 128 + l * 4; LRF(ea, L_ERED) = dy[0]; LRF(ea, L_ERED + 64) = dy[1]; }
      __syncthreads();
      if (w == 0) {
        float y = LRF(lds0 + rw_ * 4 + ft * 64, L_ERED);
#pragma unroll
        for (int i = 1; i < 8; i++) y += LRF(lds0 + rw_ * 4 + ft * 64, L_ERED + i * 128);
        const float upd = rbf(rbf(y) * sg);
        const float res = fmaf(gbf, rbf(hv + upd), xv_);
        __builtin_amdgcn_raw_buffer_store_b16((u16)(cvtpk(res, res) & 0xffffu), ro, l < 32 ? (ft ? sel[1] : sel[0]) * 1024 + fc * 2 : 0x80000000u, 0, 0);
        if (fb == 0 && l == 0) *(GAS u64*)(a.ws + WS_EPOCH) = ep;
      }
    }
    STAMP(24);
    STAMP_FLUSH(28);

  }
}

extern "C" void kernel_launch(void* const* d_in, const int* in_sizes, int n_in, void* d_out, int out_size, void* d_ws, size_t ws_size, hipStream_t stream) {
  Args a;
  a.x = (const u16*)d_in[0]; a.wqkv = (const u16*)d_in[1]; a.wo = (const u16*)d_in[2]; a.wr = (const u16*)d_in[4];
  a.wg = (const u16*)d_in[5]; a.wu = (const u16*)d_in[6]; a.wd = (const u16*)d_in[7]; a.c = (const u16*)d_in[8]; a.wm = (const u16*)d_in[9];
  a.out = (u16*)d_out; a.ws = (char*)d_ws;
  (void)hipFuncSetAttribute((const void*)mega, hipFuncAttributeMaxDynamicSharedMemorySize, L_TOTAL);
  mega<<<256, 512, L_TOTAL, stream>>>(a);
}
```
